```python
import torch

def _softmax(x, axis=-1):
    return torch.softmax(x, dim=axis)
def _gelu(x):
    return torch.nn.functional.gelu(x, approximate="tanh")
def _silu(x):
    return torch.nn.functional.silu(x)
def _sigmoid(x):
    return torch.sigmoid(x)
def _layernorm(x, axis=-1, eps=1e-5):
    m = x.mean(dim=axis, keepdim=True)
    v = x.var(dim=axis, keepdim=True, unbiased=False)
    return (x - m) / torch.sqrt(v + eps)
def _scatter_add(tgt, idx, src, axis=0):
    return tgt.clone().index_add_(axis, idx, src)
def _segment_sum(src, seg, n):
    out = torch.zeros((n,) + src.shape[1:], dtype=src.dtype)
    return out.index_add_(0, seg, src)
def _pad(x, pw):
    flat = tuple(v for pair in reversed(pw) for v in pair)
    return torch.nn.functional.pad(x, flat)

kernel_name = 'recurrent_scan__sliding_local_dilated__swiglu_ffn__prompt'

def setup_inputs():

    _w = (lambda a: (lambda t: t.to(torch.bfloat16) if t.is_floating_point() else t)(torch.as_tensor(a)))
    def _A_setup():
        B, S, D = 1, 2048, 448
        return {
            "x": _w((lambda *s: torch.randn(*s, dtype=torch.bfloat16) * 0.05)(B, S, D)),
            "wqkv": _w((lambda *s: torch.randn(*s, dtype=torch.bfloat16) * 0.05)(D, 3 * D)),
            "wo": _w((lambda *s: torch.randn(*s, dtype=torch.bfloat16) * 0.05)(D, D)),
        }
    _A_in = _A_setup()

    def _B_setup():
        B, S, D, F = 1, 2048, 448, 1792
        return {
            "x": _w((lambda *s: torch.randn(*s, dtype=torch.bfloat16) * 0.05)(B, S, D)),
            "w_gate": _w((lambda *s: torch.randn(*s, dtype=torch.bfloat16) * 0.05)(D, F)),
            "w_up": _w((lambda *s: torch.randn(*s, dtype=torch.bfloat16) * 0.05)(D, F)),
            "w_down": _w((lambda *s: torch.randn(*s, dtype=torch.bfloat16) * 0.05)(F, D)),
        }
    _B_in = _B_setup()

    return {
        "A_x": _A_in["x"],
        "A_wqkv": _A_in["wqkv"],
        "A_wo": _A_in["wo"],
        "B_x": _B_in["x"],
        "B_w_gate": _B_in["w_gate"],
        "B_w_up": _B_in["w_up"],
        "B_w_down": _B_in["w_down"],
    }

def reference(A_x, A_wqkv, A_wo, B_x, B_w_gate, B_w_up, B_w_down):
    def _A_ref(x, wqkv, wo):
        B, S, D = x.shape
        H = 16; Dh = D // H
        W, dil = 8, 2
        qkv = (lambda x, *d: x.permute(*d))((x @ wqkv).reshape(B, S, 3, H, Dh), 2, 0, 3, 1, 4)
        q, k, v = qkv[0], qkv[1], qkv[2]
        scale = Dh ** -0.5
        pos = torch.arange(S)
        off = torch.arange(W) * dil
        idx = pos[:, None] - off[None, :]
        mask = idx >= 0
        idx_c = torch.where(mask, idx, 0).reshape(-1)
        kg = k[:, :, idx_c, :].reshape(B, H, S, W, Dh)
        vg = v[:, :, idx_c, :].reshape(B, H, S, W, Dh)
        logits = torch.einsum("bhsd,bhswd->bhsw", q, kg) * scale
        logits = torch.where(mask[None, None, :, :], logits, -1e9)
        attn = _softmax(logits, axis=-1)
        o = torch.einsum("bhsw,bhswd->bhsd", attn, vg)
        return (lambda x, *d: x.permute(*d))(o, 0, 2, 1, 3).reshape(B, S, D) @ wo

    def _B_ref(x, w_gate, w_up, w_down):
        g = _silu(x @ w_gate)
        u = x @ w_up
        return (g * u) @ w_down

    x = A_x
    xn = _layernorm(x, axis=-1)
    s = _A_ref(xn, A_wqkv, A_wo)
    g = _silu(_B_ref(xn, B_w_gate, B_w_up, B_w_down))
    return x + s * g
```

```cpp
#include <hip/hip_runtime.h>
#include <hip/hip_bf16.h>
typedef unsigned short u16;
__device__ __forceinline__ float bf2f(u16 b){ return __builtin_bit_cast(float,(unsigned)b<<16);}
__device__ __forceinline__ u16 f2bf(float f){ __hip_bfloat16 h = __float2bfloat16(f); return __builtin_bit_cast(u16,h);}
__device__ __forceinline__ float rbf(float f){ return bf2f(f2bf(f)); }
constexpr int S=2048,D=448,F=1792,H=16,DH=28;

__global__ void k_ln(const u16* x, u16* xn){
  int r=blockIdx.x, l=threadIdx.x; float v[7]; float s=0;
  for(int i=0;i<7;i++){v[i]=bf2f(x[r*D+l+64*i]); s+=v[i];}
  for(int o=32;o;o>>=1) s+=__shfl_xor(s,o);
  float m=s/D; float q=0;
  for(int i=0;i<7;i++){float d=v[i]-m; q+=d*d;}
  for(int o=32;o;o>>=1) q+=__shfl_xor(q,o);
  float rs=1.0f/sqrtf(q/D+1e-5f);
  for(int i=0;i<7;i++) xn[r*D+l+64*i]=f2bf((v[i]-m)*rs);
}
__global__ void k_gemm(const u16* A,const u16* B,float* C,int K,int N){
  int n=blockIdx.x*64+threadIdx.x; int m=blockIdx.y*4+threadIdx.y;
  float acc=0; for(int k=0;k<K;k++) acc+=bf2f(A[m*K+k])*bf2f(B[k*N+n]);
  C[m*N+n]=acc;
}
__global__ void k_attn(const float* qkv,u16* o){
  int s=blockIdx.x; int h=threadIdx.x; if(h>=H) return;
  float lg[8]; float mx=-1e30f;
  for(int w=0;w<8;w++){int p=s-2*w; float a=0; if(p>=0){for(int d=0;d<DH;d++) a+=rbf(qkv[s*1344+h*DH+d])*rbf(qkv[p*1344+448+h*DH+d]); a*=0.1889822365f;} else a=-1e9f; lg[w]=a; mx=fmaxf(mx,a);}
  float sum=0; for(int w=0;w<8;w++){lg[w]=__expf(lg[w]-mx); sum+=lg[w];}
  for(int d=0;d<DH;d++){float a=0; for(int w=0;w<8;w++){int p=s-2*w; if(p<0)p=0; a+=lg[w]/sum*rbf(qkv[p*1344+896+h*DH+d]);} o[s*D+h*DH+d]=f2bf(a);}
}
__global__ void k_h(const float* g,const float* u,u16* h){
  int i=blockIdx.x*256+threadIdx.x; float a=g[i]; h[i]=f2bf(a/(1.f+__expf(-a))*u[i]);
}
__global__ void k_out(const u16* x,const float* s,const float* f,u16* out){
  int i=blockIdx.x*256+threadIdx.x; float a=f[i]; out[i]=f2bf(bf2f(x[i])+s[i]*(a/(1.f+__expf(-a))));
}
extern "C" void kernel_launch(void* const* d_in, const int* in_sizes, int n_in, void* d_out, int out_size, void* d_ws, size_t ws_size, hipStream_t stream) {
  const u16* x=(const u16*)d_in[0]; const u16* wqkv=(const u16*)d_in[1]; const u16* wo=(const u16*)d_in[2];
  const u16* wg=(const u16*)d_in[4]; const u16* wu=(const u16*)d_in[5]; const u16* wd=(const u16*)d_in[6];
  char* ws=(char*)d_ws; size_t MB=1<<20;
  u16* xn=(u16*)ws; float* qkv=(float*)(ws+4*MB); float* g=(float*)(ws+32*MB); float* u=(float*)(ws+64*MB);
  u16* o=(u16*)(ws+96*MB); u16* h=(u16*)(ws+104*MB); float* s=(float*)(ws+128*MB); float* f=(float*)(ws+144*MB);
  k_ln<<<S,64,0,stream>>>(x,xn);
  k_gemm<<<dim3(1344/64,S/4),dim3(64,4),0,stream>>>(xn,wqkv,qkv,D,1344);
  k_gemm<<<dim3(F/64,S/4),dim3(64,4),0,stream>>>(xn,wg,g,D,F);
  k_gemm<<<dim3(F/64,S/4),dim3(64,4),0,stream>>>(xn,wu,u,D,F);
  k_attn<<<S,64,0,stream>>>(qkv,o);
  k_h<<<S*F/256,256,0,stream>>>(g,u,h);
  k_gemm<<<dim3(D/64,S/4),dim3(64,4),0,stream>>>(o,wo,s,D,D);
  k_gemm<<<dim3(D/64,S/4),dim3(64,4),0,stream>>>(h,wd,f,F,D);
  k_out<<<S*D/256,256,0,stream>>>(x,s,f,(u16*)d_out);
}
```

```cpp
#include <hip/hip_runtime.h>
#include <stdint.h>
#include <utility>

#define GAS __attribute__((address_space(1)))
#define LAS __attribute__((address_space(3)))
typedef unsigned v4u __attribute__((ext_vector_type(4)));
typedef short v4s __attribute__((ext_vector_type(4)));
typedef short bf16x8 __attribute__((ext_vector_type(8)));
typedef float f32x4 __attribute__((ext_vector_type(4)));
typedef float f32x16 __attribute__((ext_vector_type(16)));
typedef float v2f __attribute__((ext_vector_type(2)));
typedef __bf16 v2bf __attribute__((ext_vector_type(2)));
typedef unsigned long long u64;

namespace {
constexpr unsigned FSTR = 4224;
constexpr unsigned WS_F1 = 65536, WS_F2 = WS_F1 + 16 * FSTR, WS_F3 = WS_F2 + 16 * FSTR;
constexpr unsigned WS_ST = 32768;
constexpr unsigned WS_QKV = 327680, QKV_T = 168 * 512;
constexpr unsigned WS_HO = WS_QKV + 64 * QKV_T, HO_T = 288 * 512;
constexpr unsigned WS_END = WS_HO + 64 * HO_T;
constexpr unsigned WS_DBG = 32u << 20;
constexpr u64 TAG_STEP = 0x9E3779B97F4A7C15ull;
constexpr int AUX_SC1 = 16;
constexpr unsigned SLOT = 40960, UNIT = 16384;
constexpr unsigned L_CSA = 122880, L_CSB = L_CSA + 3072, L_CNT = L_CSB + 2048;
constexpr unsigned CODE_BYTES = 20480;
constexpr unsigned OOB = 0x40000000u;
constexpr unsigned bunit(int s) { return (s == 0 ? 8 : s == 1 ? 9 : s == 2 ? 0 : s == 3 ? 1 : s == 4 ? 3 : s == 5 ? 4 : 5) * UNIT; }
constexpr unsigned wunit(int i) {
  return i == 18 ? 7 * UNIT : (i % 9) == 0 ? 2 * UNIT : (i % 9) == 1 ? 6 * UNIT : bunit(i % 9 - 2);
}
constexpr int PRO = 10;
constexpr int G_ST = 19, G_POLL = 50, G_ATT = 54, G_F1 = 42;
constexpr int avail(int g) {
  return g < 2 ? 20 : g < 6 ? 25 : g < 10 ? 30 : g < 14 ? 35 : g < 18 ? 39 : g < 22 ? 43 : g < 26 ? 49 : 53 + 2 * ((g - 26) / 4);
}
constexpr int cum(int g) {
  int c = PRO;
  for (int k = 0; k <= g; ++k) {
    int n = c + (k < 28 ? ((k & 1) && k != 27 ? 1 : 2) : 1);
    c = n < avail(k) ? n : avail(k);
  }
  return c;
}
static_assert(cum(55) == 67 && cum(27) == 53, "every item issued; B6 and W1 in front of the first store");
constexpr int rew(int g) { return g == 36 || g == 37 ? 9 : g == 40 || g == 41 ? 10 : g == 48 || g == 49 ? 11 : -1; }
constexpr int rreg(int g) { return g == 33 ? 0 : g == 37 ? 1 : g == 45 ? 2 : g == 49 ? 3 : -1; }
constexpr int nst(int g) { return g == 28 || g == 29 || g == 31 || g == 33 ? 1 : 0; }
constexpr int nextra(int g) {
  return (g == G_ST) + (rew(g) >= 0) + (g == G_ATT ? 3 : g == G_ATT + 1 ? 2 : 0);
}
constexpr int nops(int g) {
  int c = PRO + 3;
  for (int k = 0; k <= g; ++k) c += nst(k) + cum(k) - cum(k - 1) + nextra(k);
  return c;
}
constexpr int rankI(int it) {
  if (it < PRO) return it < 5 ? it + 1 : it + 2;
  int g = 0;
  while (cum(g) <= it) ++g;
  return nops(g - 1) + nst(g) + it - cum(g - 1);
}
constexpr int imin(int a, int b) { return a < b ? a : b; }
constexpr unsigned wbase(int g) {
  return g < 28 ? (((g >> 2) & 2) && g >= 0 ? 2 * SLOT + 16384 : 16384) : g < 36 ? 8 * UNIT : g < 44 ? 0 : 3 * UNIT;
}
constexpr bool isW(int it) { return it == 47 || it == 48 || it >= 51; }

template <int N> using IC = std::integral_constant<int, N>;
template <int... I, class F> __device__ __forceinline__ void sfor_(std::integer_sequence<int, I...>, F&& f) { (f(std::integral_constant<int, I>{}), ...); }
template <int N, class F> __device__ __forceinline__ void sfor(F&& f) { sfor_(std::make_integer_sequence<int, N>{}, f); }

#define WAITL0() asm volatile("s_waitcnt lgkmcnt(0)" ::: "memory")
#define BAR() do { asm volatile("" ::: "memory"); __builtin_amdgcn_s_barrier(); asm volatile("" ::: "memory"); } while (0)
#define SCHED() __builtin_amdgcn_sched_barrier(0)
#define PRIO(n) __builtin_amdgcn_s_setprio(n)
template <class T> __device__ __forceinline__ void TIE(T& v) { asm volatile("" : "+v"(v)); }
template <int N> __device__ __forceinline__ void waitv() { asm volatile("s_waitcnt vmcnt(%0)" ::"i"(N) : "memory"); }

typedef __amdgpu_buffer_rsrc_t rsrc_t;
__device__ __forceinline__ rsrc_t mkrs(const char* p, unsigned bytes) { return __builtin_amdgcn_make_buffer_rsrc((void*)p, 0, bytes, 0x00020000); }
template <unsigned L, unsigned S> __device__ __forceinline__ void dmac(unsigned voff, rsrc_t rs, unsigned ldsw) {
  unsigned t;
  asm volatile("s_add_u32 m0, %3, %4\n\ts_mov_b32 %0, %5\n\tbuffer_load_dwordx4 %1, %2, %0 offen lds"
               : "=&s"(t) : "v"(voff), "s"(rs), "s"(ldsw), "i"(L), "i"(S) : "memory", "scc");
}
template <unsigned L, unsigned S> __device__ __forceinline__ void dmam(unsigned voff, rsrc_t rs, unsigned ldsw, unsigned pitch) {
  unsigned t;
  asm volatile("s_add_u32 m0, %3, %4\n\ts_mul_i32 %0, %6, %5\n\tbuffer_load_dwordx4 %1, %2, %0 offen lds"
               : "=&s"(t) : "v"(voff), "s"(rs), "s"(ldsw), "i"(L), "i"(S), "s"(pitch) : "memory", "scc");
}
template <unsigned L, unsigned S, unsigned A> __device__ __forceinline__ void dmaa(unsigned voff, rsrc_t rs, unsigned ldsw, unsigned pitch) {
  unsigned t;
  asm volatile("s_add_u32 m0, %3, %4\n\ts_mul_i32 %0, %6, %5\n\ts_add_u32 %0, %0, %7\n\tbuffer_load_dwordx4 %1, %2, %0 offen lds"
               : "=&s"(t) : "v"(voff), "s"(rs), "s"(ldsw), "i"(L), "i"(S), "s"(pitch), "i"(A) : "memory", "scc");
}
template <unsigned S> __device__ __forceinline__ void ld16(v4u& d, unsigned voff, rsrc_t rs) {
  unsigned t;
  asm volatile("s_mov_b32 %1, %4\n\tbuffer_load_dwordx4 %0, %2, %3, %1 offen sc1" : "=v"(d), "=&s"(t) : "v"(voff), "s"(rs), "i"(S) : "memory");
}
template <unsigned S0, unsigned S1> __device__ __forceinline__ void ld16x2c(v4u& d0, v4u& d1, unsigned voff, rsrc_t rs, unsigned c) {
  unsigned t;
  asm volatile("s_cmp_eq_u32 %7, 0\n\ts_cbranch_scc1 1f\n\ts_mov_b32 %2, %5\n\tbuffer_load_dwordx4 %0, %3, %4, %2 offen sc1\n\t"
               "s_mov_b32 %2, %6\n\tbuffer_load_dwordx4 %1, %3, %4, %2 offen sc1\n1:"
               : "=&v"(d0), "=&v"(d1), "=&s"(t) : "v"(voff), "s"(rs), "i"(S0), "i"(S1), "s"(c) : "memory", "scc");
}
template <unsigned L, unsigned S, unsigned A> __device__ __forceinline__ void dmaac(unsigned voff, rsrc_t rs, unsigned ldsw, unsigned pitch, unsigned c) {
  unsigned t;
  asm volatile("s_cmp_eq_u32 %8, 0\n\ts_cbranch_scc1 1f\n\ts_add_u32 m0, %3, %4\n\ts_mul_i32 %0, %6, %5\n\ts_add_u32 %0, %0, %7\n\t"
               "buffer_load_dwordx4 %1, %2, %0 offen lds\n1:"
               : "=&s"(t) : "v"(voff), "s"(rs), "s"(ldsw), "i"(L), "i"(S), "s"(pitch), "i"(A), "s"(c) : "memory", "scc");
}
template <int N1, int N0> __device__ __forceinline__ void waitc(unsigned c) {
  static_assert(N0 <= N1 && N0 >= 0, "");
  asm volatile("s_cmp_lg_u32 %0, 0\n\ts_cbranch_scc1 1f\n\ts_waitcnt vmcnt(%2)\n1:\n\ts_waitcnt vmcnt(%1)" ::"s"(c), "i"(N1), "i"(N0) : "memory", "scc");
}
__device__ __forceinline__ void ld8(u64& d, unsigned voff, rsrc_t rs) {
  asm volatile("buffer_load_dwordx2 %0, %1, %2, 0 offen sc1" : "=v"(d) : "v"(voff), "s"(rs) : "memory");
}
__device__ __forceinline__ float wave_sum63(float v) {
#define DPP_ADD(ctrl, rm) v += __builtin_bit_cast(float, __builtin_amdgcn_update_dpp(0, __builtin_bit_cast(int, v), ctrl, rm, 0xf, true));
  DPP_ADD(0x111, 0xf) DPP_ADD(0x112, 0xf) DPP_ADD(0x114, 0xf) DPP_ADD(0x118, 0xf) DPP_ADD(0x142, 0xa) DPP_ADD(0x143, 0xc)
#undef DPP_ADD
  return v;
}
__device__ __forceinline__ bf16x8 ldtr(unsigned a) {
  v4s lo = __builtin_amdgcn_ds_read_tr16_b64_v4i16((LAS v4s*)(uintptr_t)a);
  v4s hi = __builtin_amdgcn_ds_read_tr16_b64_v4i16((LAS v4s*)(uintptr_t)(a + 512));
  return __builtin_shufflevector(lo, hi, 0, 1, 2, 3, 4, 5, 6, 7);
}
__device__ __forceinline__ unsigned pk(float a, float b) {
  v2f f = {a, b};
  return __builtin_bit_cast(unsigned, __builtin_convertvector(f, v2bf));
}
__device__ __forceinline__ float asf(unsigned d) { return __builtin_bit_cast(float, d); }
__device__ __forceinline__ float blo(unsigned d) { return __builtin_bit_cast(float, d << 16); }
__device__ __forceinline__ float bhi(unsigned d) { return __builtin_bit_cast(float, d & 0xffff0000u); }
__device__ __forceinline__ f32x16 mm32(bf16x8 a, bf16x8 b, f32x16 c) { return __builtin_amdgcn_mfma_f32_32x32x16_bf16(a, b, c, 0, 0, 0); }
__device__ __forceinline__ f32x4 mm16(bf16x8 a, bf16x8 b, f32x4 c) { return __builtin_amdgcn_mfma_f32_16x16x32_bf16(a, b, c, 0, 0, 0); }
__device__ __forceinline__ bf16x8 asb(v4u v) { return __builtin_bit_cast(bf16x8, v); }

__device__ __forceinline__ unsigned asu(float f) { return __builtin_bit_cast(unsigned, f); }
template <bool MAX> __device__ __forceinline__ float rows4(float v) {
  {
    const auto r = __builtin_amdgcn_permlane16_swap(asu(v), asu(v), false, false);
    const unsigned a = r[0], b = r[1];
    v = MAX ? fmaxf(asf(a), asf(b)) : asf(a) + asf(b);
  }
  const auto r = __builtin_amdgcn_permlane32_swap(asu(v), asu(v), false, false);
  const unsigned a = r[0], b = r[1];
  return MAX ? fmaxf(asf(a), asf(b)) : asf(a) + asf(b);
}
__device__ __forceinline__ void arrive(unsigned cnt, unsigned foff, rsrc_t rs, u64 tag) {
  unsigned a = cnt, t;
  u64 tv = tag;
  asm volatile("s_mov_b64 exec, 1\n\tv_mov_b32 %0, 1\n\tds_add_rtn_u32 %0, %1, %0\n\ts_waitcnt lgkmcnt(0)\n\t"
               "v_cmp_eq_u32 vcc, 7, %0\n\ts_cbranch_vccz 1f\n\tbuffer_store_dwordx2 %2, off, %3, %4 sc1\n1:\n\ts_mov_b64 exec, -1"
               : "=&v"(t) : "v"(a), "v"(tv), "s"(rs), "s"(foff) : "memory", "vcc");
}
__device__ __forceinline__ void ld8c(u64& d, unsigned voff, rsrc_t rs, unsigned c) {
  asm volatile("s_cmp_eq_u32 %3, 0\n\ts_cbranch_scc1 1f\n\tbuffer_load_dwordx2 %0, %1, %2, 0 offen sc1\n1:" : "=&v"(d) : "v"(voff), "s"(rs), "s"(c) : "memory", "scc");
}
__device__ __forceinline__ void poll(u64 v, unsigned voff, rsrc_t rs, u64 tag, unsigned c) {
  unsigned n = 1u << 19;
  asm volatile("s_cmp_eq_u32 %5, 0\n\ts_cbranch_scc1 2f\n\ts_waitcnt vmcnt(0)\n1:\n\tv_cmp_ne_u64 vcc, %4, %0\n\ts_cbranch_vccz 2f\n\ts_sleep 3\n\t"
               "buffer_load_dwordx2 %0, %2, %3, 0 offen sc1\n\ts_sub_u32 %1, %1, 1\n\ts_waitcnt vmcnt(0)\n\ts_cbranch_scc0 1b\n2:"
               : "+v"(v), "+s"(n) : "v"(voff), "s"(rs), "s"(tag), "s"(c) : "memory", "vcc", "scc");
}
__device__ __forceinline__ void waitw(unsigned lds) {
  unsigned a = lds, t, n = 1u << 22;
  asm volatile("1:\n\tds_read_b32 %0, %2\n\ts_waitcnt lgkmcnt(0)\n\tv_cmp_eq_u32 vcc, 0, %0\n\ts_cbranch_vccz 2f\n\ts_sleep 1\n\t"
               "s_sub_u32 %1, %1, 1\n\ts_cbranch_scc0 1b\n2:"
               : "=&v"(t), "+s"(n) : "v"(a) : "memory", "vcc", "scc");
}
__device__ __forceinline__ void pollg(v4u& v, unsigned voff, rsrc_t rs, u64 tag) {
  unsigned n = 1u << 20, v1 = v[1], v3 = v[3], t;
  const unsigned lo = (unsigned)tag, hi = (unsigned)(tag >> 32);
  if (__all(v1 == lo && v3 == hi)) return;
#pragma unroll 1
  for (; n; --n) {
    v = __builtin_amdgcn_raw_buffer_load_b128(rs, voff, 0, AUX_SC1);
    v1 = v[1]; v3 = v[3];
    if (__all(v1 == lo && v3 == hi)) break;
    __builtin_amdgcn_s_sleep(1);
  }
  (void)t;
}
__device__ __forceinline__ bf16x8 csfrag(unsigned lds) {
  const f32x4 p = *(const LAS f32x4*)(uintptr_t)lds;
  const float c = (p[0] + p[1]) + (p[2] + p[3]);
  const unsigned h = pk(c, 0.f);
  const unsigned d = pk(c, c - blo(h));
  return asb(v4u{d, d, 0u, 0u});
}
template <int C> __device__ __forceinline__ void store_q(const f32x16& a, float rstd, rsrc_t rs, unsigned off) {
  v4u o;
#pragma unroll
  for (int e = 0; e < 4; ++e) o[e] = pk(a[8 * C + 2 * e] * rstd, a[8 * C + 2 * e + 1] * rstd);
  __builtin_amdgcn_raw_buffer_store_b128(o, rs, off + C * 1024, 0, AUX_SC1);
}
template <int Q> __device__ __forceinline__ void swiglu4(const f32x16& g, const f32x16& u, float rstd, v4u& o) {
  const float k1 = -1.4426950408889634f * rstd, k2 = rstd * rstd;
  float h[4];
#pragma unroll
  for (int e = 0; e < 4; ++e) {
    const float gg = g[4 * Q + e];
    const float r = __builtin_amdgcn_rcpf(1.0f + __builtin_amdgcn_exp2f(gg * k1));
    h[e] = (gg * k2) * (u[4 * Q + e] * r);
  }
  o[2 * (Q & 1)] = pk(h[0], h[1]);
  o[2 * (Q & 1) + 1] = pk(h[2], h[3]);
}
#define STAMP(i)
#define CSTAMP(i)
#define STAMPB(i)
#define STAMPN(i) STAMP(i)
}

__global__ __launch_bounds__(512, 2) void fused_block(const char* __restrict__ x, const char* __restrict__ wqkv, const char* __restrict__ wo,
                                                      const char* __restrict__ wg, const char* __restrict__ wu, const char* __restrict__ wd,
                                                      char* __restrict__ out, char* __restrict__ ws) {
  STAMP(0);
  asm volatile("" ::"s"(x), "s"(wqkv), "s"(wo), "s"(wg), "s"(wu), "s"(wd), "s"(out), "s"(ws));
  const int tid = threadIdx.x, lane = tid & 63, w = __builtin_amdgcn_readfirstlane(tid >> 6);
  const int rg = w & 3, set = w >> 2, rr = rg ^ (2 * set);
  const int b = blockIdx.x, xl = b & 7, idx = b >> 3;
  const int j = (idx & 3) < 3 ? 3 * (xl & 3) + (idx & 3) : 12 + (xl & 3), p = 8 * (xl >> 2) + (idx >> 2);
  const bool typeA = j < 12;
  const bool p2 = idx < 28;
  const int tok = lane & 31, hh = lane >> 5;
  const unsigned lw1 = w * 1024, lw2 = w * 2048;
  const rsrc_t rs = mkrs(ws, WS_END), rx = mkrs(x, 2048 * 896);

  const int ts = 128 * p + 8 * j + w;
  v4u xrow;
  unsigned warm;
  asm volatile("buffer_load_dwordx4 %0, %1, %2, 0 offen" : "=v"(xrow) : "v"(lane < 56 ? ts * 896 + lane * 16 : OOB), "s"(rx) : "memory");
  const unsigned l3 = lane >> 3, l7 = lane & 7;
  const unsigned cw = (l7 ^ (((lane >> 4) & 1) << 2)) * 16;
  const unsigned vx0 = (128 * p + 16 * w + l3) * 896 + ((l7 ^ (lane >> 4)) * 16), vx1 = (vx0 ^ 64) + 8 * 896;
  const int qb0 = typeA ? j : (j == 15 ? 18 : 3 * j - 24);
  const unsigned vq = (8 * w + l3) * 2688 + qb0 * 128 + cw, vf = (8 * w + l3) * 3584 + j * 128 + cw, v12 = typeA ? vf : vq;
  const rsrc_t rq = mkrs(wqkv, 448 * 2688), rgt = mkrs(wg, 448 * 3584), rup = mkrs(wu, 448 * 3584);
  const rsrc_t r1 = mkrs(typeA ? wg : wqkv + 128, typeA ? 448 * 3584 : 448 * 2688 - 128);
  const rsrc_t r2 = mkrs(typeA ? wu : wqkv + 256, typeA ? 448 * 3584 : 448 * 2688 - 256);
  const unsigned st12 = typeA ? 64 * 3584 : 64 * 2688;
  auto itemA = [&](auto it_) {
    constexpr int it = it_, s = it / 5, k = it % 5;
    constexpr unsigned sl = (s & 3) * SLOT;
    if constexpr (k == 0) dmac<sl, s * 128>(vx0, rx, lw2);
    else if constexpr (k == 1) dmac<sl + 1024, s * 128>(vx1, rx, lw2);
    else if constexpr (k == 2) dmac<sl + 16384, s * 64 * 2688>(vq, rq, lw1);
    else if constexpr (k == 3) dmam<sl + 24576, s>(v12, r1, lw1, st12);
    else dmam<sl + 32768, s>(v12, r2, lw1, st12);
  };
  sfor<5>([&](auto it) { itemA(it); });
  {
    u64 pc;
    asm volatile("s_getpc_b64 %0" : "=s"(pc));
    const unsigned o = ((idx * 8 + w) * 8 + (lane & 7)) * 128 % CODE_BYTES;
    asm volatile("global_load_dword %0, %1, %2" : "=v"(warm) : "v"(o), "s"(pc) : "memory");
  }
  sfor<PRO - 5>([&](auto it) { itemA(IC<5 + it>{}); });
  SCHED();
  const int rg2 = p2 ? 4 * xl + idx / 7 : 2 * p, cg = idx % 7;
  const bool owave = w >= 6;
  const unsigned cO = (w + 2) & 8, cW = cO ^ 8, cH = __builtin_amdgcn_readfirstlane(p2 ? cW : 0u), c18 = (w & 4) ^ 4, cC = (j + 4) & 16, c7 = (w + 1) & 8;
  const int ksA = owave ? 122 + 16 * (w - 6) : 8 * w;
  const int ksB = owave ? 112 + 16 * (w - 6) : 48 + (w < 5 ? 11 * w : 54);
  const unsigned pstr = owave ? 28 * 896 : 32 * 896;
  const rsrc_t rw2 = mkrs(owave ? wo : wd, owave ? 448 * 896 : 1792 * 896);
  const unsigned vdA = p2 ? (owave ? (w - 6) * 224 + 140 : ksA * 16) * 896 + l3 * 896 + cg * 128 + cw : OOB;
  const unsigned vdB = p2 ? (owave ? (w - 6) * 224 : ksB * 16) * 896 + l3 * 896 + cg * 128 + cw : OOB;
  auto issueWh = [&](auto i_, auto hf_) {
    constexpr int i = i_, hf = hf_;
    constexpr unsigned L = wunit(i) + hf * 1024, S = (i & 7) >> 1 | (i >= 16 ? 4 : 0), A = ((i & 1) * 16 + hf * 8) * 896;
    if constexpr (i == 6 || i == 7) dmaac<L, S, A>(vdA, rw2, lw2, pstr, cW);
    else if constexpr (i == 18) dmaac<L, S, A>(vdB, rw2, lw2, pstr, c18);
    else dmaa<L, S, A>(i < 8 ? vdA : vdB, rw2, lw2, pstr);
  };
  auto issueW = [&](auto i) { issueWh(i, IC<0>{}); issueWh(i, IC<1>{}); };
  auto item = [&](auto it_) {
    constexpr int it = it_;
    if constexpr (it < 35) {
      itemA(it_);
    } else if constexpr (isW(it)) {
      issueWh(IC<(it < 49 ? 0 : (it - 49) >> 1)>{}, IC<((it - 47) & 1)>{});
    } else {
      constexpr int s = it < 47 ? (it - 35) >> 1 : 6, k = (it - 35) & 1;
      dmac<bunit(s) + k * 8192, s * 64 * 3584 + 12 * 128>(vf, k ? rup : rgt, lw1);
    }
  };
  u64 tag;
  asm volatile("s_load_dwordx2 %0, %1, 0x0\n\ts_waitcnt lgkmcnt(0)" : "=s"(tag) : "s"(ws) : "memory");
  tag += TAG_STEP;
  waitv<PRO + 1>();
  TIE(xrow);
  {
    float a = 0.f, q = 0.f;
#pragma unroll
    for (int e = 0; e < 4; ++e) {
      const float lo = blo(xrow[e]), hi = bhi(xrow[e]);
      a += lo + hi; q = fmaf(lo, lo, fmaf(hi, hi, q));
    }
    a = wave_sum63(a); q = wave_sum63(q);
    const float m = a * (1.0f / 448.0f);
    const float r = __builtin_amdgcn_rsqf(fmaxf(q * (1.0f / 448.0f) - m * m, 0.f) + 1e-5f);
    const v4u gr = {__builtin_bit_cast(unsigned, m), (unsigned)tag, __builtin_bit_cast(unsigned, r), (unsigned)(tag >> 32)};
    __builtin_amdgcn_raw_buffer_store_b128(gr, rs, lane == 63 ? WS_ST + ts * 16 : OOB, 0, AUX_SC1);
  }

  const unsigned q4 = (lane & 15) >> 2, p4 = lane & 3, sp4 = ((p4 & 1) << 1) | (p4 >> 1);
  const unsigned trl = (8 * hh + q4) * 128 + (4 * ((lane >> 4) & 1) + sp4) * 8;
  const unsigned aw = (trl + ((set ^ (q4 >> 1)) << 6)) | (rr << 11);
  const unsigned ax = (32 * rg + tok) * 128 + (((2 * rr + hh) ^ ((tok >> 1) & 7)) << 4);
  const unsigned selw = (((lane >> 4) ^ (lane >> 2)) & 1) ? 0u : 0x3F803F80u;
  const bf16x8 sel = asb(v4u{selw, selw, selw, selw});
  unsigned bw[4], bx[4];
#pragma unroll
  for (int i = 0; i < 4; ++i) { bw[i] = (aw ^ (i * 2048)) + 16384; bx[i] = ax ^ (i * 32); TIE(bw[i]); TIE(bx[i]); }

  bf16x8 xf[28];
  f32x16 acc[3], accB[2];
  f32x4 cs[3], csB[2];
#pragma unroll
  for (int t = 0; t < 3; ++t) { acc[t] = 0.f; cs[t] = 0.f; }
#pragma unroll
  for (int t = 0; t < 2; ++t) { accB[t] = 0.f; csB[t] = 0.f; }
  bf16x8 wf[3][3];
  auto rdX = [&](auto n_) {
    constexpr int n = n_, s = n >> 2, i = n & 3;
    if constexpr (s == 2 || s == 6) { bx[i] += 2 * SLOT; TIE(bx[i]); }
    if constexpr (s == 4) { bx[i] -= 2 * SLOT; TIE(bx[i]); }
    xf[n] = *(const LAS bf16x8*)(uintptr_t)(bx[i] + (s & 1) * SLOT);
  };
  auto rdW = [&](auto g_, auto t_) {
    constexpr int g = g_, t = t_, i = g & 3;
    if constexpr (t == 0 && wbase(g) != wbase(g - 4)) { bw[i] += wbase(g) - wbase(g - 4); TIE(bw[i]); }
    constexpr unsigned off = (g < 28 ? ((g >> 2) & 3) * SLOT + 16384 : bunit((g - 28) >> 2)) + t * 8192 - wbase(g);
    static_assert(off + 512 < 65536, "ds offset");
    wf[g % 3][t] = ldtr(bw[i] + off);
  };
  const unsigned tt1 = 4 * p + rg;
  const unsigned vl = lane * 16;
  v4u stg;
  float rstd;
  bf16x8 mu, cf[3];

  waitv<PRO - 5 + 1>();
  TIE(warm);
  BAR();
  STAMP(1); CSTAMP(12);
  sfor<2>([&](auto n) { rdX(n); rdW(n, IC<0>{}); rdW(n, IC<1>{}); rdW(n, IC<2>{}); });
  sfor<28>([&](auto n_) {
    constexpr int n = n_, s = n >> 2, i = n & 3, c0 = cum(n - 1), c1 = cum(n);
    constexpr int NT = n < 26 ? 3 : 2;
    SCHED();
    if constexpr (i != 2) PRIO(3 - ((i + 2) & 3));
    acc[0] = mm32(wf[n % 3][0], xf[n], acc[0]);
    SCHED();
    if constexpr (i == 2) {
      if constexpr (s == 4) { if (tid < 4) *(LAS unsigned*)(uintptr_t)(L_CNT + tid * 4) = 0u; }
      if constexpr (s == 6) {
#pragma unroll
        for (int t = 0; t < 3; ++t) *(LAS float*)(uintptr_t)(L_CSA + ((set * 3 + t) * 32 + tok) * 16 + rg * 4) = cs[t][0];
      }
      WAITL0();
      static_assert(c0 > (s < 6 ? 5 * s + 9 : 36), "next stage issued");
      constexpr int y = nops(n - 1) - 1 - rankI(s < 6 ? 5 * s + 9 : 36);
      waitv<s == 6 ? imin(y, nops(n - 1) - nops(G_ST)) : y>();
      BAR();
      PRIO(3);
    }
    if constexpr (n < 26) rdX(IC<n + 2>{});
    rdW(IC<n + 2>{}, IC<0>{});
    if constexpr (i == 1) { rdW(IC<n + 2>{}, IC<1>{}); rdW(IC<n + 2>{}, IC<2>{}); }
    if constexpr (n == 26) {
      TIE(stg);
      pollg(stg, WS_ST + (tt1 * 32 + tok) * 16, rs, tag);
      rstd = asf(stg[2]);
      const float m = asf(stg[0]);
      const unsigned mh = pk(-m, -m);
      const float ml = -m - blo(mh);
      mu = asb(v4u{hh ? 0u : mh, hh ? 0u : pk(ml, ml), 0u, 0u});
      TIE(mu);
    }
    if constexpr (n == 27) { cf[2] = csfrag(L_CSA + ((set * 3 + 2) * 32 + tok) * 16); TIE(cf[2]); }
    SCHED();
    if constexpr (i == 0) cs[0] = mm16(sel, wf[n % 3][0], cs[0]);
    if constexpr (n == 27) acc[0] = mm32(cf[0], mu, acc[0]);
    acc[1] = mm32(wf[n % 3][1], xf[n], acc[1]);
    SCHED();
    if constexpr (i != 1) rdW(IC<n + 2>{}, IC<1>{});
    if constexpr (c1 > c0) item(IC<c0>{});
    if constexpr (n == 26) { cf[0] = csfrag(L_CSA + ((set * 3 + 0) * 32 + tok) * 16); TIE(cf[0]); }
    SCHED();
    if constexpr (i == 0) cs[1] = mm16(sel, wf[n % 3][1], cs[1]);
    if constexpr (n == 27) acc[1] = mm32(cf[1], mu, acc[1]);
    acc[2] = mm32(wf[n % 3][2], xf[n], acc[2]);
    SCHED();
    if constexpr (i != 1 && NT == 3) rdW(IC<n + 2>{}, IC<2>{});
    if constexpr (c1 > c0 + 1) item(IC<c0 + 1>{});
    if constexpr (n == G_ST) ld16<WS_ST>(stg, (tt1 * 32 + tok) * 16, rs);
    if constexpr (n == 26) { cf[1] = csfrag(L_CSA + ((set * 3 + 1) * 32 + tok) * 16); TIE(cf[1]); }
    SCHED();
    if constexpr (i == 0) cs[2] = mm16(sel, wf[n % 3][2], cs[2]);
    if constexpr (n == 27) acc[2] = mm32(cf[2], mu, acc[2]);
  });
  SCHED();
  STAMP(2); CSTAMP(13);
  const unsigned oQ = j < 15 ? vl + WS_QKV + tt1 * QKV_T + (2 * qb0 + set) * 2048 : OOB;
  const unsigned oH = vl + WS_HO + tt1 * HO_T + (2 * j + set) * 2048;
  const int n16 = lane & 15, g4 = lane >> 4, odd = j & 1;
  const int tq = 128 * p + 16 * w + n16, tk = tq >= 16 ? tq - 16 : tq;
  const unsigned oq = (tq >> 5) * QKV_T + (tq & 31) * 16 + (g4 + ((7 * j) >> 1)) * 512;
  const unsigned ok = (tk >> 5) * QKV_T + (tk & 31) * 16 + (g4 + ((7 * j) >> 1)) * 512;
  const unsigned fo1 = WS_F1 + (g4 == 1 && p > 0 ? p - 1 : g4 == 2 ? rg2 >> 1 : p) * FSTR + n16 * 8;
  const unsigned a2 = w * 2048 + trl + ((q4 >> 1) << 6);
  u64 fl;
  v4u qf, k0, k1, v0, v1, hv;
  bf16x8 w2[9][2];
  auto toReg = [&](auto i) {
    w2[i][0] = ldtr(a2 + wunit(i)); w2[i][1] = ldtr((a2 ^ 64) + wunit(i));
  };
  sfor<28>([&](auto n_) {
    constexpr int n = n_, s = n >> 2, i = n & 3, g = 28 + n, c0 = cum(g - 1), c1 = cum(g);
    SCHED();
    if constexpr (i != 2) PRIO(3 - ((i + 2) & 3));
    accB[0] = mm32(wf[g % 3][0], xf[n], accB[0]);
    SCHED();
    if constexpr (i == 2) {
      if constexpr (s == 6) {
#pragma unroll
        for (int t = 0; t < 2; ++t) *(LAS float*)(uintptr_t)(L_CSB + ((set * 2 + t) * 32 + tok) * 16 + rg * 4) = csB[t][0];
      }
      WAITL0();
      if constexpr (g == G_F1) STAMPB(6);
      if constexpr (s == 0) waitv<nops(g - 1) - 1 - rankI(48)>();
      if constexpr (s == 1) {
        waitc<nops(g - 1) - 1 - rankI(52) + 2, nops(g - 1) - 1 - rankI(52)>(cC);
      }
      if constexpr (g == G_F1) waitv<nops(g - 1) - 1 - nops(32)>();
      if constexpr (g == 46) waitv<nops(g - 1) - 1 - rankI(56)>();
      if constexpr (g == G_ATT) { STAMPB(8); poll(fl, fo1, rs, tag, c7); }
      BAR();
      if constexpr (g == G_ATT) STAMPB(9);
      PRIO(3);
      if constexpr (s == 1) STAMPB(5);
      if constexpr (g == G_F1) {
        STAMPB(7);
        if (tid == 0) __hip_atomic_store((GAS u64*)(ws + (WS_F1 + p * FSTR + j * 8)), tag, __ATOMIC_RELAXED, __HIP_MEMORY_SCOPE_AGENT);
      }
    }
    if constexpr (n < 26) {
      rdW(IC<g + 2>{}, IC<0>{});
      if constexpr (i == 1) rdW(IC<g + 2>{}, IC<1>{});
    }
    if constexpr (rreg(g) >= 0) toReg(IC<rreg(g)>{});
    if constexpr (n == 0) store_q<0>(acc[0], rstd, rs, oQ);
    if constexpr (n == 1) store_q<1>(acc[0], rstd, rs, oQ);
    if constexpr (n >= 2 && n <= 5) {
      constexpr int Q = n - 2;
      if (typeA) {
        swiglu4<Q>(acc[1], acc[2], rstd, hv);
        if constexpr (Q & 1) __builtin_amdgcn_raw_buffer_store_b128(hv, rs, oH + (Q >> 1) * 1024, 0, AUX_SC1);
        else TIE(hv);
      } else store_q<(Q & 1)>(acc[1 + (Q >> 1)], rstd, rs, oQ + (1 + (Q >> 1)) * 4096);
    }
    if constexpr (n == 26) { cf[0] = csfrag(L_CSB + ((set * 2 + 0) * 32 + tok) * 16); TIE(cf[0]); }
    if constexpr (n == 27) { cf[1] = csfrag(L_CSB + ((set * 2 + 1) * 32 + tok) * 16); TIE(cf[1]); }
    SCHED();
    if constexpr (i == 0) csB[0] = mm16(sel, wf[g % 3][0], csB[0]);
    if constexpr (n == 27) accB[0] = mm32(cf[0], mu, accB[0]);
    accB[1] = mm32(wf[g % 3][1], xf[n], accB[1]);
    SCHED();
    if constexpr (n < 26 && i != 1) rdW(IC<g + 2>{}, IC<1>{});
    sfor<c1 - c0>([&](auto k) { item(IC<c0 + k>{}); });
    if constexpr (rew(g) >= 0) issueWh(IC<rew(g)>{}, IC<(g & 1)>{});
    if constexpr (g == G_ATT) {
      ld16<WS_QKV>(qf, oq, rs);
      ld16<WS_QKV + 56 * 512>(k0, ok, rs);
      ld16<WS_QKV + 56 * 512>(k1, oq, rs);
    }
    if constexpr (g == G_ATT + 1) { ld16<WS_QKV + 112 * 512>(v0, ok, rs); ld16<WS_QKV + 112 * 512>(v1, oq, rs); }
    if constexpr (g == G_POLL) ld8c(fl, fo1, rs, c7);
    SCHED();
    if constexpr (i == 0) csB[1] = mm16(sel, wf[g % 3][1], csB[1]);
    if constexpr (n == 27) accB[1] = mm32(cf[1], mu, accB[1]);
  });
  SCHED();
  PRIO(0);
  STAMP(4); CSTAMP(14);
  const unsigned vA = vl + 2 * rg2 * HO_T + ksA * 1024, vB = vl + 2 * rg2 * HO_T + ksB * 1024;
  v4u af[19][2];
  auto ldE = [&](auto i_, unsigned c) {
    constexpr int i = i_;
    ld16x2c<WS_HO + i * 1024, WS_HO + HO_T + i * 1024>(af[i][0], af[i][1], vA, rs, c);
  };
  auto ldL = [&](auto i_) {
    constexpr int i = i_;
    if constexpr (i == 18) ld16x2c<WS_HO + (i - 8) * 1024, WS_HO + HO_T + (i - 8) * 1024>(af[i][0], af[i][1], vB, rs, c18);
    else {
      ld16<WS_HO + (i - 8) * 1024>(af[i][0], vB, rs);
      ld16<WS_HO + HO_T + (i - 8) * 1024>(af[i][1], vB, rs);
    }
  };
  swiglu4<0>(accB[0], accB[1], rstd, hv); swiglu4<1>(accB[0], accB[1], rstd, hv);
  __builtin_amdgcn_raw_buffer_store_b128(hv, rs, oH + 24 * 2048, 0, AUX_SC1);
  swiglu4<2>(accB[0], accB[1], rstd, hv); swiglu4<3>(accB[0], accB[1], rstd, hv);
  __builtin_amdgcn_raw_buffer_store_b128(hv, rs, oH + 24 * 2048 + 1024, 0, AUX_SC1);
  SCHED();
  STAMPN(5);
  sfor<3>([&](auto i) { ldE(i, cH); });
  waitc<2 + 6, 2>(cH);
  TIE(qf); TIE(k0); TIE(k1); TIE(v0); TIE(v1);
  {
    if (odd && g4 == 0) { qf[0] = 0; qf[1] = 0; }
    if (!odd && g4 == 3) { qf[2] = 0; qf[3] = 0; }
    const unsigned scr = wunit(3) + w * 2048;
    *(LAS v4u*)(uintptr_t)(scr + n16 * 64 + g4 * 16) = v0;
    *(LAS v4u*)(uintptr_t)(scr + 1024 + n16 * 64 + g4 * 16) = v1;
    const f32x4 z4 = 0.f;
    const f32x4 sa = mm16(asb(k0), asb(qf), z4), sb = mm16(asb(k1), asb(qf), z4);
    bf16x8 vt[2];
#pragma unroll
    for (int t = 0; t < 2; ++t) {
      const unsigned a = scr + (4 * g4 + q4) * 64 + (((2 * p4 + t + odd) * 8) & 63);
      const v4s lo = __builtin_amdgcn_ds_read_tr16_b64_v4i16((LAS v4s*)(uintptr_t)a);
      const v4s hi = __builtin_amdgcn_ds_read_tr16_b64_v4i16((LAS v4s*)(uintptr_t)(a + 1024));
      vt[t] = __builtin_shufflevector(lo, hi, 0, 1, 2, 3, 4, 5, 6, 7);
    }
    sfor<5>([&](auto i) { toReg(IC<4 + i>{}); });
    const bool par = n16 & 1, c0 = n16 - 4 * g4 >= 0, c1 = n16 - 4 * g4 >= 2, back = tq >= 16;
    const float ninf = -3.0e38f, sc = 0.1889822365046136f * 1.4426950408889634f;
    const float s0 = c0 ? (par ? sb[1] : sb[0]) * sc : back ? (par ? sa[1] : sa[0]) * sc : ninf;
    const float s1 = c1 ? (par ? sb[3] : sb[2]) * sc : back ? (par ? sa[3] : sa[2]) * sc : ninf;
    const float mx = rows4<true>(fmaxf(s0, s1));
    const float e0 = __builtin_amdgcn_exp2f(s0 - mx), e1 = __builtin_amdgcn_exp2f(s1 - mx);
    const float inv = __builtin_amdgcn_rcpf(rows4<false>(e0 + e1));
    const unsigned u0 = par ? pk(0.f, e0 * inv) : pk(e0 * inv, 0.f), u1 = par ? pk(0.f, e1 * inv) : pk(e1 * inv, 0.f);
    const v4u pf = {c0 ? 0u : u0, c1 ? 0u : u1, c0 ? u0 : 0u, c1 ? u1 : 0u};
    const f32x4 oa = mm16(vt[0], asb(pf), z4), ob = mm16(vt[1], asb(pf), z4);
    v4u ov = {pk(oa[0], oa[1]), pk(oa[2], oa[3]), pk(ob[0], ob[1]), pk(ob[2], ob[3])};
    if (g4 == 3) { ov[2] = 0; ov[3] = 0; }
    __builtin_amdgcn_raw_buffer_store_b128(ov, rs, (tq >> 5) * HO_T + (tq & 31) * 16 + g4 * 512 + WS_HO + (224 + 4 * j) * 512, 0, AUX_SC1);
  }
  SCHED();
  STAMPN(6);
  sfor<3>([&](auto i) { ldE(IC<3 + i>{}, cH); });
  WAITL0();
  issueW(IC<12>{});
  waitc<6 + 1 + 6 + 2, 1 + 2>(cH);
  arrive(L_CNT, WS_F2 + p * FSTR + j * 8, rs, tag);
  STAMPN(7);
  sfor<2>([&](auto i) { ldE(IC<6 + i>{}, cH); });
  issueW(IC<13>{});
  waitc<6 + 2 + 4 + 2, 2 + 2>(cH);
  arrive(L_CNT + 4, WS_F3 + p * FSTR + j * 8, rs, tag);
  if (!p2) {
    if (b == 255 && w == 0) {
#pragma unroll 1
      for (int k = 0; k < 4; ++k) poll(~tag, WS_F1 + (k * 4 + g4) * FSTR + n16 * 8, rs, tag, 1u);
      if (lane == 0) __hip_atomic_store((GAS u64*)ws, tag, __ATOMIC_RELAXED, __HIP_MEMORY_SCOPE_AGENT);
    }
    return;
  }

  {
    const unsigned fo2 = (w == 6 ? WS_F3 : WS_F2) + (rg2 >> 1) * FSTR + n16 * 8;
    const unsigned vo = (64 * rg2 + 8 * w + l3) * 896 + cg * 128 + l7 * 16;
    v4u xres;
    f32x16 c2[2][2];
#pragma unroll
    for (int t = 0; t < 4; ++t) c2[t >> 1][t & 1] = 0.f;
    auto step = [&](auto i_) {
      constexpr int i = i_;
      bf16x8 wa, wb;
      if constexpr (i < 9) { wa = w2[i][0]; wb = w2[i][1]; }
      else { wa = ldtr(a2 + wunit(i)); wb = ldtr((a2 ^ 64) + wunit(i)); }
      TIE(af[i][0]); TIE(af[i][1]);
      c2[0][0] = mm32(wa, asb(af[i][0]), c2[0][0]);
      c2[0][1] = mm32(wa, asb(af[i][1]), c2[0][1]);
      c2[1][0] = mm32(wb, asb(af[i][0]), c2[1][0]);
      c2[1][1] = mm32(wb, asb(af[i][1]), c2[1][1]);
      SCHED();
    };
    ld16<0>(xres, vo, rx);
    issueW(IC<14>{});
    if (!owave) { step(IC<0>{}); step(IC<1>{}); }
    issueW(IC<15>{});
    if (!owave) { step(IC<2>{}); waitv<2 + 4 + 2 + 1 + 4>(); step(IC<3>{}); }
    issueW(IC<16>{});
    if (!owave) { step(IC<4>{}); step(IC<5>{}); }
    issueW(IC<17>{});
    issueW(IC<18>{});
    if (!owave) { waitv<2 + 1 + 4 + 4>(); step(IC<6>{}); step(IC<7>{}); }
    poll(~tag, fo2, rs, tag, cO);
    if (owave) *(LAS unsigned*)(uintptr_t)(L_CNT + 8 + (w & 1) * 4) = 1u;
    waitw(L_CNT + (owave ? 8 : 12));
    STAMPN(8);
    sfor<6>([&](auto i) { ldE(i, cO); });
    sfor<4>([&](auto i) { ldL(IC<8 + i>{}); });
    sfor<6>([&](auto i) {
      if (owave) { waitv<18>(); step(i); }
      ldL(IC<12 + i>{});
    });
    ldL(IC<18>{});
    sfor<10>([&](auto i) { waitv<2 * (9 - i)>(); step(IC<8 + i>{}); });
    if (w < 4) step(IC<18>{});
    STAMPN(9);
#pragma unroll
    for (int nt = 0; nt < 2; ++nt)
#pragma unroll
      for (int tt = 0; tt < 2; ++tt)
#pragma unroll
        for (int c = 0; c < 2; ++c) {
          v4u o;
#pragma unroll
          for (int e = 0; e < 4; ++e) o[e] = pk(c2[nt][tt][8 * c + 2 * e], c2[nt][tt][8 * c + 2 * e + 1]);
          *(LAS v4u*)(uintptr_t)((2 * nt + c) * UNIT + w * 2048 + hh * 1024 + (((32 * tt + tok) ^ (4 * (2 * c + hh))) * 16)) = o;
        }
    WAITL0();
    BAR();
    STAMP(10);
    float f[8], sv[8];
#pragma unroll
    for (int e = 0; e < 8; ++e) { f[e] = 0.f; sv[e] = 0.f; }
    const unsigned ar = (l7 >> 1) * UNIT + (l7 & 1) * 1024 + (((8 * w + l3) ^ (4 * (l7 & 3))) * 16);
#pragma unroll
    for (int k = 0; k < 8; ++k) {
      const v4u v = *(const LAS v4u*)(uintptr_t)(ar + k * 2048);
#pragma unroll
      for (int e = 0; e < 4; ++e) {
        if (k < 6) { f[2 * e] += blo(v[e]); f[2 * e + 1] += bhi(v[e]); }
        else { sv[2 * e] += blo(v[e]); sv[2 * e + 1] += bhi(v[e]); }
      }
    }
    TIE(xres);
    float r[8];
#pragma unroll
    for (int e = 0; e < 8; ++e) {
      const float sg = f[e] * __builtin_amdgcn_rcpf(1.0f + __builtin_amdgcn_exp2f(f[e] * -1.4426950408889634f));
      r[e] = fmaf(sv[e], sg, (e & 1) ? bhi(xres[e >> 1]) : blo(xres[e >> 1]));
    }
    const v4u o = {pk(r[0], r[1]), pk(r[2], r[3]), pk(r[4], r[5]), pk(r[6], r[7])};
    *(GAS v4u*)(out + vo) = o;
    STAMP(11); CSTAMP(15);
  }
}

extern "C" void kernel_launch(void* const* d_in, const int* in_sizes, int n_in, void* d_out, int out_size, void* d_ws, size_t ws_size,
                              hipStream_t stream) {
  (void)in_sizes; (void)n_in; (void)out_size; (void)ws_size;
  (void)hipFuncSetAttribute((const void*)fused_block, hipFuncAttributeMaxDynamicSharedMemorySize, 163840);
  fused_block<<<256, 512, 163840, stream>>>((const char*)d_in[0], (const char*)d_in[1], (const char*)d_in[2], (const char*)d_in[4],
                                            (const char*)d_in[5], (const char*)d_in[6], (char*)d_out, (char*)d_ws);
}
```

```cpp
#include <hip/hip_runtime.h>
#include <stdint.h>
#include <utility>

#define GAS __attribute__((address_space(1)))
#define LAS __attribute__((address_space(3)))
typedef unsigned v4u __attribute__((ext_vector_type(4)));
typedef short v4s __attribute__((ext_vector_type(4)));
typedef short bf16x8 __attribute__((ext_vector_type(8)));
typedef float f32x4 __attribute__((ext_vector_type(4)));
typedef float f32x16 __attribute__((ext_vector_type(16)));
typedef float v2f __attribute__((ext_vector_type(2)));
typedef __bf16 v2bf __attribute__((ext_vector_type(2)));
typedef unsigned long long u64;

namespace {
constexpr unsigned WS_F1 = 2048, WS_F2 = 4096;
constexpr unsigned WS_ST = 32768;
constexpr unsigned WS_QKV = 65536, QKV_T = 168 * 512;
constexpr unsigned WS_HO = WS_QKV + 64 * QKV_T, HO_T = 288 * 512;
constexpr unsigned WS_END = WS_HO + 64 * HO_T;
constexpr unsigned WS_DBG = 32u << 20;
constexpr u64 TAG_STEP = 0x9E3779B97F4A7C15ull;
constexpr int AUX_SC1 = 16;
constexpr unsigned SLOT = 40960, UNIT = 16384;
constexpr unsigned L_CSA = 122880, L_CSB = 114688;
constexpr unsigned CODE_BYTES = 20480;
constexpr unsigned OOB = 0x40000000u;
constexpr unsigned bunit(int s) { return (s == 0 ? 8 : s == 1 ? 9 : s == 2 ? 0 : s == 3 ? 1 : s == 4 ? 3 : s == 5 ? 4 : 5) * UNIT; }
constexpr unsigned wunit(int i) {
  return i == 18 ? 7 * UNIT : (i % 9) == 0 ? 2 * UNIT : (i % 9) == 1 ? 6 * UNIT : bunit(i % 9 - 2);
}
constexpr int PRO = 10;
constexpr int avail(int g) {
  return g < 3 ? 20 : g < 7 ? 25 : g < 11 ? 30 : g < 15 ? 35 : g < 19 ? 39 : g < 23 ? 43 : g < 27 ? 49 : 53 + 2 * ((g - 27) / 4);
}
constexpr int cum(int g) {
  int c = PRO;
  for (int k = 0; k <= g; ++k) {
    int n = c + (k < 28 ? ((k & 1) ? 1 : 2) : k == 55 ? 2 : 1);
    c = n < avail(k) ? n : avail(k);
  }
  return c;
}
static_assert(cum(55) == 67, "every item issued");
constexpr unsigned wbase(int g) {
  return g < 28 ? (((g >> 2) & 2) && g >= 0 ? 2 * SLOT + 16384 : 16384) : g < 36 ? 8 * UNIT : g < 44 ? 0 : 3 * UNIT;
}
constexpr bool isW(int it) { return it == 47 || it == 48 || it >= 51; }
constexpr int younger(int last, int issued, bool withW) {
  int c = 0;
  for (int it = last + 1; it < issued; ++it) c += (isW(it) && !withW) ? 0 : 1;
  return c;
}

template <int N> using IC = std::integral_constant<int, N>;
template <int... I, class F> __device__ __forceinline__ void sfor_(std::integer_sequence<int, I...>, F&& f) { (f(std::integral_constant<int, I>{}), ...); }
template <int N, class F> __device__ __forceinline__ void sfor(F&& f) { sfor_(std::make_integer_sequence<int, N>{}, f); }

#define WAITL0() asm volatile("s_waitcnt lgkmcnt(0)" ::: "memory")
#define BAR() do { asm volatile("" ::: "memory"); __builtin_amdgcn_s_barrier(); asm volatile("" ::: "memory"); } while (0)
#define SCHED() __builtin_amdgcn_sched_barrier(0)
template <class T> __device__ __forceinline__ void TIE(T& v) { asm volatile("" : "+v"(v)); }
template <int N> __device__ __forceinline__ void waitv() { asm volatile("s_waitcnt vmcnt(%0)" ::"i"(N) : "memory"); }

typedef __amdgpu_buffer_rsrc_t rsrc_t;
__device__ __forceinline__ rsrc_t mkrs(const char* p, unsigned bytes) { return __builtin_amdgcn_make_buffer_rsrc((void*)p, 0, bytes, 0x00020000); }
template <unsigned L, unsigned S> __device__ __forceinline__ void dmac(unsigned voff, rsrc_t rs, unsigned ldsw) {
  unsigned t;
  asm volatile("s_add_u32 m0, %3, %4\n\ts_mov_b32 %0, %5\n\tbuffer_load_dwordx4 %1, %2, %0 offen lds"
               : "=&s"(t) : "v"(voff), "s"(rs), "s"(ldsw), "i"(L), "i"(S) : "memory", "scc");
}
template <unsigned L, unsigned S> __device__ __forceinline__ void dmam(unsigned voff, rsrc_t rs, unsigned ldsw, unsigned pitch) {
  unsigned t;
  asm volatile("s_add_u32 m0, %3, %4\n\ts_mul_i32 %0, %6, %5\n\tbuffer_load_dwordx4 %1, %2, %0 offen lds"
               : "=&s"(t) : "v"(voff), "s"(rs), "s"(ldsw), "i"(L), "i"(S), "s"(pitch) : "memory", "scc");
}
template <unsigned L, unsigned S, unsigned A> __device__ __forceinline__ void dmaa(unsigned voff, rsrc_t rs, unsigned ldsw, unsigned pitch) {
  unsigned t;
  asm volatile("s_add_u32 m0, %3, %4\n\ts_mul_i32 %0, %6, %5\n\ts_add_u32 %0, %0, %7\n\tbuffer_load_dwordx4 %1, %2, %0 offen lds"
               : "=&s"(t) : "v"(voff), "s"(rs), "s"(ldsw), "i"(L), "i"(S), "s"(pitch), "i"(A) : "memory", "scc");
}
template <unsigned S> __device__ __forceinline__ void ld16(v4u& d, unsigned voff, rsrc_t rs) {
  unsigned t;
  asm volatile("s_mov_b32 %1, %4\n\tbuffer_load_dwordx4 %0, %2, %3, %1 offen sc1" : "=v"(d), "=&s"(t) : "v"(voff), "s"(rs), "i"(S) : "memory");
}
__device__ __forceinline__ void ld8(u64& d, unsigned voff, rsrc_t rs) {
  asm volatile("buffer_load_dwordx2 %0, %1, %2, 0 offen sc1" : "=v"(d) : "v"(voff), "s"(rs) : "memory");
}
__device__ __forceinline__ float wave_sum63(float v) {
#define DPP_ADD(ctrl, rm) v += __builtin_bit_cast(float, __builtin_amdgcn_update_dpp(0, __builtin_bit_cast(int, v), ctrl, rm, 0xf, true));
  DPP_ADD(0x111, 0xf) DPP_ADD(0x112, 0xf) DPP_ADD(0x114, 0xf) DPP_ADD(0x118, 0xf) DPP_ADD(0x142, 0xa) DPP_ADD(0x143, 0xc)
#undef DPP_ADD
  return v;
}
__device__ __forceinline__ bf16x8 ldtr(unsigned a) {
  v4s lo = __builtin_amdgcn_ds_read_tr16_b64_v4i16((LAS v4s*)(uintptr_t)a);
  v4s hi = __builtin_amdgcn_ds_read_tr16_b64_v4i16((LAS v4s*)(uintptr_t)(a + 512));
  return __builtin_shufflevector(lo, hi, 0, 1, 2, 3, 4, 5, 6, 7);
}
__device__ __forceinline__ unsigned pk(float a, float b) {
  v2f f = {a, b};
  return __builtin_bit_cast(unsigned, __builtin_convertvector(f, v2bf));
}
__device__ __forceinline__ float asf(unsigned d) { return __builtin_bit_cast(float, d); }
__device__ __forceinline__ float blo(unsigned d) { return __builtin_bit_cast(float, d << 16); }
__device__ __forceinline__ float bhi(unsigned d) { return __builtin_bit_cast(float, d & 0xffff0000u); }
__device__ __forceinline__ f32x16 mm32(bf16x8 a, bf16x8 b, f32x16 c) { return __builtin_amdgcn_mfma_f32_32x32x16_bf16(a, b, c, 0, 0, 0); }
__device__ __forceinline__ f32x4 mm16(bf16x8 a, bf16x8 b, f32x4 c) { return __builtin_amdgcn_mfma_f32_16x16x32_bf16(a, b, c, 0, 0, 0); }
__device__ __forceinline__ bf16x8 asb(v4u v) { return __builtin_bit_cast(bf16x8, v); }

__device__ __forceinline__ void poll(const char* ws, unsigned off, u64 tag) {
#pragma unroll 1
  for (unsigned spins = 0; spins < (1u << 20); ++spins) {
    const u64 v = __hip_atomic_load((const GAS u64*)(ws + off), __ATOMIC_RELAXED, __HIP_MEMORY_SCOPE_AGENT);
    if (__all(v == tag)) break;
    __builtin_amdgcn_s_sleep(2);
  }
  __builtin_amdgcn_fence(__ATOMIC_ACQUIRE, "wavefront");
}
__device__ __forceinline__ bf16x8 csfrag(unsigned lds) {
  const f32x4 p = *(const LAS f32x4*)(uintptr_t)lds;
  const float c = (p[0] + p[1]) + (p[2] + p[3]);
  const unsigned h = pk(c, 0.f);
  const unsigned d = pk(c, c - blo(h));
  return asb(v4u{d, d, 0u, 0u});
}
template <int C> __device__ __forceinline__ void store_q(const f32x16& a, float rstd, rsrc_t rs, unsigned off) {
  v4u o;
#pragma unroll
  for (int e = 0; e < 4; ++e) o[e] = pk(a[8 * C + 2 * e] * rstd, a[8 * C + 2 * e + 1] * rstd);
  __builtin_amdgcn_raw_buffer_store_b128(o, rs, off + C * 1024, 0, AUX_SC1);
}
template <int Q> __device__ __forceinline__ void swiglu4(const f32x16& g, const f32x16& u, float rstd, v4u& o) {
  const float k1 = -1.4426950408889634f * rstd, k2 = rstd * rstd;
  float h[4];
#pragma unroll
  for (int e = 0; e < 4; ++e) {
    const float gg = g[4 * Q + e];
    const float r = __builtin_amdgcn_rcpf(1.0f + __builtin_amdgcn_exp2f(gg * k1));
    h[e] = (gg * k2) * (u[4 * Q + e] * r);
  }
  o[2 * (Q & 1)] = pk(h[0], h[1]);
  o[2 * (Q & 1) + 1] = pk(h[2], h[3]);
}
#define STAMP(i)
#define CSTAMP(i)
#define STAMPB(i)
#define STAMPN(i) STAMP(i)
}

__global__ __launch_bounds__(512, 2) void fused_block(const char* __restrict__ x, const char* __restrict__ wqkv, const char* __restrict__ wo,
                                                      const char* __restrict__ wg, const char* __restrict__ wu, const char* __restrict__ wd,
                                                      char* __restrict__ out, char* __restrict__ ws) {
  STAMP(0);
  asm volatile("" ::"s"(x), "s"(wqkv), "s"(wo), "s"(wg), "s"(wu), "s"(wd), "s"(out), "s"(ws));
  const int tid = threadIdx.x, lane = tid & 63, w = __builtin_amdgcn_readfirstlane(tid >> 6);
  const int rg = w & 3, set = w >> 2, rr = rg ^ (2 * set);
  const int b = blockIdx.x, xl = b & 7, idx = b >> 3;
  const int j = 4 * (xl & 3) + (idx & 3), p = 8 * (xl >> 2) + (idx >> 2);
  const bool typeA = j < 12;
  const bool p2 = idx < 28;
  const int tok = lane & 31, hh = lane >> 5;
  const unsigned lw1 = w * 1024, lw2 = w * 2048;
  const rsrc_t rs = mkrs(ws, WS_END), rx = mkrs(x, 2048 * 896);

  const int ts = 128 * p + 8 * j + w;
  v4u xrow;
  unsigned warm;
  asm volatile("buffer_load_dwordx4 %0, %1, %2, 0 offen" : "=v"(xrow) : "v"(lane < 56 ? ts * 896 + lane * 16 : OOB), "s"(rx) : "memory");
  const unsigned l3 = lane >> 3, l7 = lane & 7;
  const unsigned cw = (l7 ^ (((lane >> 4) & 1) << 2)) * 16;
  const unsigned vx0 = (128 * p + 16 * w + l3) * 896 + ((l7 ^ (lane >> 4)) * 16), vx1 = (vx0 ^ 64) + 8 * 896;
  const int qb0 = typeA ? j : (j == 15 ? 18 : 3 * j - 24);
  const unsigned vq = (8 * w + l3) * 2688 + qb0 * 128 + cw, vf = (8 * w + l3) * 3584 + j * 128 + cw, v12 = typeA ? vf : vq;
  const rsrc_t rq = mkrs(wqkv, 448 * 2688), rgt = mkrs(wg, 448 * 3584), rup = mkrs(wu, 448 * 3584);
  const rsrc_t r1 = mkrs(typeA ? wg : wqkv + 128, typeA ? 448 * 3584 : 448 * 2688 - 128);
  const rsrc_t r2 = mkrs(typeA ? wu : wqkv + 256, typeA ? 448 * 3584 : 448 * 2688 - 256);
  const unsigned st12 = typeA ? 64 * 3584 : 64 * 2688;
  auto itemA = [&](auto it_) {
    constexpr int it = it_, s = it / 5, k = it % 5;
    constexpr unsigned sl = (s & 3) * SLOT;
    if constexpr (k == 0) dmac<sl, s * 128>(vx0, rx, lw2);
    else if constexpr (k == 1) dmac<sl + 1024, s * 128>(vx1, rx, lw2);
    else if constexpr (k == 2) dmac<sl + 16384, s * 64 * 2688>(vq, rq, lw1);
    else if constexpr (k == 3) dmam<sl + 24576, s>(v12, r1, lw1, st12);
    else dmam<sl + 32768, s>(v12, r2, lw1, st12);
  };
  sfor<5>([&](auto it) { itemA(it); });
  {
    u64 pc;
    asm volatile("s_getpc_b64 %0" : "=s"(pc));
    const unsigned o = ((idx * 8 + w) * 8 + (lane & 7)) * 128 % CODE_BYTES;
    asm volatile("global_load_dword %0, %1, %2" : "=v"(warm) : "v"(o), "s"(pc) : "memory");
  }
  sfor<PRO - 5>([&](auto it) { itemA(IC<5 + it>{}); });
  SCHED();
  const int rg2 = p2 ? 4 * xl + idx / 7 : 2 * p, cg = idx % 7;
  const bool owave = w >= 6;
  const int nk = owave ? 16 : (w >= 4 ? 18 : 19);
  const int ksA = owave ? 112 + 16 * (w - 6) : 8 * w;
  const int ksB = owave ? ksA : 40 + (w < 5 ? 11 * w : 54);
  const unsigned pstr = owave ? 28 * 896 : 32 * 896;
  const rsrc_t rw2 = mkrs(owave ? wo : wd, owave ? 448 * 896 : 1792 * 896);
  const unsigned vdA = (owave ? (w - 6) * 224 : ksA * 16) * 896 + l3 * 896 + cg * 128 + cw;
  const unsigned vdB = (owave ? (w - 6) * 224 : ksB * 16) * 896 + l3 * 896 + cg * 128 + cw;
  auto issueWh = [&](auto i_, auto hf_) {
    constexpr int i = i_, hf = hf_;
    dmaa<wunit(i) + hf * 1024, (i >> 1), ((i & 1) * 16 + hf * 8) * 896>(i < 8 ? vdA : vdB, rw2, lw2, pstr);
  };
  auto issueW = [&](auto i) { issueWh(i, IC<0>{}); issueWh(i, IC<1>{}); };
  auto item = [&](auto it_) {
    constexpr int it = it_;
    if constexpr (it < 35) {
      itemA(it_);
    } else if constexpr (isW(it)) {
      if (p2) issueWh(IC<(it < 49 ? 0 : (it - 49) >> 1)>{}, IC<((it - 47) & 1)>{});
    } else {
      constexpr int s = it < 47 ? (it - 35) >> 1 : 6, k = (it - 35) & 1;
      dmac<bunit(s) + k * 8192, s * 64 * 3584 + 12 * 128>(vf, k ? rup : rgt, lw1);
    }
  };
  u64 tag;
  asm volatile("s_load_dwordx2 %0, %1, 0x0\n\ts_waitcnt lgkmcnt(0)" : "=s"(tag) : "s"(ws) : "memory");
  tag += TAG_STEP;
  waitv<PRO + 1>();
  TIE(xrow);
  {
    float a = 0.f, q = 0.f;
#pragma unroll
    for (int e = 0; e < 4; ++e) {
      const float lo = blo(xrow[e]), hi = bhi(xrow[e]);
      a += lo + hi; q = fmaf(lo, lo, fmaf(hi, hi, q));
    }
    a = wave_sum63(a); q = wave_sum63(q);
    const float m = a * (1.0f / 448.0f);
    const float r = __builtin_amdgcn_rsqf(fmaxf(q * (1.0f / 448.0f) - m * m, 0.f) + 1e-5f);
    const v4u gr = {__builtin_bit_cast(unsigned, m), (unsigned)tag, __builtin_bit_cast(unsigned, r), (unsigned)(tag >> 32)};
    __builtin_amdgcn_raw_buffer_store_b128(gr, rs, lane == 63 ? WS_ST + ts * 16 : OOB, 0, AUX_SC1);
  }

  const unsigned q4 = (lane & 15) >> 2, p4 = lane & 3, sp4 = ((p4 & 1) << 1) | (p4 >> 1);
  const unsigned trl = (8 * hh + q4) * 128 + (4 * ((lane >> 4) & 1) + sp4) * 8;
  const unsigned aw = (trl + ((set ^ (q4 >> 1)) << 6)) | (rr << 11);
  const unsigned ax = (32 * rg + tok) * 128 + (((2 * rr + hh) ^ ((tok >> 1) & 7)) << 4);
  const unsigned selw = (((lane >> 4) ^ (lane >> 2)) & 1) ? 0u : 0x3F803F80u;
  const bf16x8 sel = asb(v4u{selw, selw, selw, selw});
  unsigned bw[4], bx[4];
#pragma unroll
  for (int i = 0; i < 4; ++i) { bw[i] = (aw ^ (i * 2048)) + 16384; bx[i] = ax ^ (i * 32); TIE(bw[i]); TIE(bx[i]); }

  bf16x8 xf[28];
  f32x16 acc[3], accB[2];
  f32x4 cs[3], csB[2];
#pragma unroll
  for (int t = 0; t < 3; ++t) { acc[t] = 0.f; cs[t] = 0.f; }
#pragma unroll
  for (int t = 0; t < 2; ++t) { accB[t] = 0.f; csB[t] = 0.f; }
  bf16x8 wf[2][3];
  auto rdX = [&](auto n_) {
    constexpr int n = n_, s = n >> 2, i = n & 3;
    if constexpr (s == 2 || s == 6) { bx[i] += 2 * SLOT; TIE(bx[i]); }
    if constexpr (s == 4) { bx[i] -= 2 * SLOT; TIE(bx[i]); }
    xf[n] = *(const LAS bf16x8*)(uintptr_t)(bx[i] + (s & 1) * SLOT);
  };
  auto rdW = [&](auto g_, auto t_) {
    constexpr int g = g_, t = t_, i = g & 3;
    if constexpr (t == 0 && wbase(g) != wbase(g - 4)) { bw[i] += wbase(g) - wbase(g - 4); TIE(bw[i]); }
    constexpr unsigned off = (g < 28 ? ((g >> 2) & 3) * SLOT + 16384 : bunit((g - 28) >> 2)) + t * 8192 - wbase(g);
    static_assert(off + 512 < 65536, "ds offset");
    wf[g & 1][t] = ldtr(bw[i] + off);
  };
  const unsigned tt1 = 4 * p + rg;
  const unsigned vl = lane * 16;
  v4u stg;
  constexpr int G_ST = 21;

  waitv<PRO - 5 + 1>();
  TIE(warm);
  BAR();
  STAMP(1); CSTAMP(12);
  rdX(IC<0>{}); rdW(IC<0>{}, IC<0>{}); rdW(IC<0>{}, IC<1>{}); rdW(IC<0>{}, IC<2>{});
  sfor<28>([&](auto n_) {
    constexpr int n = n_, s = n >> 2, i = n & 3, c0 = cum(n - 1), c1 = cum(n);
    constexpr int NT = n < 27 ? 3 : 2;
    SCHED();
    acc[0] = mm32(wf[n & 1][0], xf[n], acc[0]);
    SCHED();
    if constexpr (i == 3) {
      if constexpr (s == 6) {
#pragma unroll
        for (int t = 0; t < 3; ++t) *(LAS float*)(uintptr_t)(L_CSA + ((set * 3 + t) * 32 + tok) * 16 + rg * 4) = cs[t][0];
      }
      WAITL0();
      static_assert(c0 > (s < 6 ? 5 * s + 9 : 36), "next stage issued");
      constexpr int y = younger(s < 6 ? 5 * s + 9 : 36, c0, false) + (s == 0 ? 1 : 0), ys = younger(cum(G_ST) - 1, c0, false);
      waitv<(s == 6 && ys < y) ? ys : y>();
      BAR();
    }
    if constexpr (n < 27) rdX(IC<n + 1>{});
    rdW(IC<n + 1>{}, IC<0>{});
    if constexpr (i == 2) { rdW(IC<n + 1>{}, IC<1>{}); rdW(IC<n + 1>{}, IC<2>{}); }
    SCHED();
    if constexpr (i == 0) cs[0] = mm16(sel, wf[n & 1][0], cs[0]);
    acc[1] = mm32(wf[n & 1][1], xf[n], acc[1]);
    SCHED();
    if constexpr (i != 2) rdW(IC<n + 1>{}, IC<1>{});
    if constexpr (c1 > c0) item(IC<c0>{});
    SCHED();
    if constexpr (i == 0) cs[1] = mm16(sel, wf[n & 1][1], cs[1]);
    acc[2] = mm32(wf[n & 1][2], xf[n], acc[2]);
    SCHED();
    if constexpr (i != 2 && NT == 3) rdW(IC<n + 1>{}, IC<2>{});
    if constexpr (c1 > c0 + 1) item(IC<c0 + 1>{});
    if constexpr (n == G_ST) ld16<WS_ST>(stg, (tt1 * 32 + tok) * 16, rs);
    SCHED();
    if constexpr (i == 0) cs[2] = mm16(sel, wf[n & 1][2], cs[2]);
  });
  SCHED();
  STAMP(2); CSTAMP(13);
  TIE(stg);
  if (!__all(stg[1] == (unsigned)tag && stg[3] == (unsigned)(tag >> 32))) {
#pragma unroll 1
    for (unsigned spins = 0; spins < (1u << 20); ++spins) {
      stg = __builtin_amdgcn_raw_buffer_load_b128(rs, WS_ST + (tt1 * 32 + tok) * 16, 0, AUX_SC1);
      if (__all(stg[1] == (unsigned)tag && stg[3] == (unsigned)(tag >> 32))) break;
      __builtin_amdgcn_s_sleep(2);
    }
  }
  const float rstd = asf(stg[2]);
  bf16x8 mu;
  {
    const float m = asf(stg[0]);
    const unsigned mh = pk(-m, -m);
    const float ml = -m - blo(mh);
    mu = asb(v4u{hh ? 0u : mh, hh ? 0u : pk(ml, ml), 0u, 0u});
  }
#pragma unroll
  for (int t = 0; t < 3; ++t) acc[t] = mm32(csfrag(L_CSA + ((set * 3 + t) * 32 + tok) * 16), mu, acc[t]);
  const unsigned oQ = j < 15 ? vl + WS_QKV + tt1 * QKV_T + (2 * qb0 + set) * 2048 : OOB;
  const unsigned oH = vl + WS_HO + tt1 * HO_T + (2 * j + set) * 2048;
  STAMP(3);
  const int n16 = lane & 15, g4 = lane >> 4, odd = j & 1;
  const int tq = 128 * p + 16 * w + n16, tk = tq >= 16 ? tq - 16 : tq;
  const unsigned oq = (tq >> 5) * QKV_T + (tq & 31) * 16 + (g4 + ((7 * j) >> 1)) * 512;
  const unsigned ok = (tk >> 5) * QKV_T + (tk & 31) * 16 + (g4 + ((7 * j) >> 1)) * 512;
  const unsigned fo1 = WS_F1 + ((g4 == 1 && p > 0 ? p - 1 : g4 == 2 ? rg2 >> 1 : p) * 16 + n16) * 8;
  u64 fl;
  v4u qf, k0, k1, v0, v1, hv;
  constexpr int G_POLL = 48, G_ATT = 52;
  sfor<28>([&](auto n_) {
    constexpr int n = n_, s = n >> 2, i = n & 3, g = 28 + n, c0 = cum(g - 1), c1 = cum(g);
    SCHED();
    accB[0] = mm32(wf[n & 1][0], xf[n], accB[0]);
    SCHED();
    if constexpr (i == 3) {
      if constexpr (s == 6) {
#pragma unroll
        for (int t = 0; t < 2; ++t) *(LAS float*)(uintptr_t)(L_CSB + ((set * 2 + t) * 32 + tok) * 16 + rg * 4) = csB[t][0];
      }
      WAITL0();
      if constexpr (s == 2) STAMPB(6);
      if constexpr (s == 0) waitv<younger(38, c0, false)>();
      if constexpr (s == 1) waitv<younger(40, c0, false)>();
      if constexpr (s == 2) {
        static_assert(cum(33) >= 51, "B6 issued in front of the last store");
        if (p2) waitv<c0 - cum(33)>(); else waitv<0>();
      }
      BAR();
      if constexpr (s == 1) STAMPB(5);
      if constexpr (s == 2) STAMPB(7);
      if constexpr (s == 2) {
        if (tid == 0) __hip_atomic_store((GAS u64*)(ws + WS_F1) + p * 16 + j, tag, __ATOMIC_RELAXED, __HIP_MEMORY_SCOPE_AGENT);
      }
    }
    if constexpr (n < 27) {
      rdW(IC<g + 1>{}, IC<0>{});
      if constexpr (i == 2) rdW(IC<g + 1>{}, IC<1>{});
    }
    if constexpr (n == 1) store_q<0>(acc[0], rstd, rs, oQ);
    if constexpr (n == 2) store_q<1>(acc[0], rstd, rs, oQ);
    if constexpr (n >= 3 && n <= 6) {
      constexpr int Q = n - 3;
      if (typeA) {
        swiglu4<Q>(acc[1], acc[2], rstd, hv);
        if constexpr (Q & 1) __builtin_amdgcn_raw_buffer_store_b128(hv, rs, oH + (Q >> 1) * 1024, 0, AUX_SC1);
        else TIE(hv);
      } else store_q<(Q & 1)>(acc[1 + (Q >> 1)], rstd, rs, oQ + (1 + (Q >> 1)) * 4096);
    }
    SCHED();
    if constexpr (i == 0) csB[0] = mm16(sel, wf[n & 1][0], csB[0]);
    accB[1] = mm32(wf[n & 1][1], xf[n], accB[1]);
    SCHED();
    if constexpr (n < 27 && i != 2) rdW(IC<g + 1>{}, IC<1>{});
    if constexpr (g == G_ATT) {
      STAMPB(8);
      if (p2) waitv<c0 - cum(G_POLL)>(); else waitv<0>();
      TIE(fl);
      if (!__all(fl == tag)) poll(ws, fo1, tag);
      STAMPB(9);
      ld16<WS_QKV>(qf, oq, rs);
      ld16<WS_QKV + 56 * 512>(k0, ok, rs);
    }
    if constexpr (g == G_ATT + 1) { ld16<WS_QKV + 56 * 512>(k1, oq, rs); ld16<WS_QKV + 112 * 512>(v0, ok, rs); }
    if constexpr (g == G_ATT + 2) ld16<WS_QKV + 112 * 512>(v1, oq, rs);
    sfor<c1 - c0>([&](auto k) { item(IC<c0 + k>{}); });
    if constexpr (g == G_POLL) ld8(fl, fo1, rs);
    SCHED();
    if constexpr (i == 0) csB[1] = mm16(sel, wf[n & 1][1], csB[1]);
  });
  SCHED();
  STAMP(4); CSTAMP(14);
#pragma unroll
  for (int t = 0; t < 2; ++t) accB[t] = mm32(csfrag(L_CSB + ((set * 2 + t) * 32 + tok) * 16), mu, accB[t]);
  swiglu4<0>(accB[0], accB[1], rstd, hv); swiglu4<1>(accB[0], accB[1], rstd, hv);
  __builtin_amdgcn_raw_buffer_store_b128(hv, rs, oH + 24 * 2048, 0, AUX_SC1);
  swiglu4<2>(accB[0], accB[1], rstd, hv); swiglu4<3>(accB[0], accB[1], rstd, hv);
  __builtin_amdgcn_raw_buffer_store_b128(hv, rs, oH + 24 * 2048 + 1024, 0, AUX_SC1);
  SCHED();
  STAMPN(5);

  waitv<2>();
  TIE(qf); TIE(k0); TIE(k1); TIE(v0); TIE(v1);
  const unsigned a2 = w * 2048 + trl + ((q4 >> 1) << 6);
  bf16x8 w2[9][2];
  sfor<9>([&](auto i) {
    unsigned c = a2 + wunit(i), d = (a2 ^ 64) + wunit(i);
    w2[i][0] = ldtr(c); w2[i][1] = ldtr(d);
  });
  {
    if (odd && g4 == 0) { qf[0] = 0; qf[1] = 0; }
    if (!odd && g4 == 3) { qf[2] = 0; qf[3] = 0; }
    const unsigned scr = wunit(0) + w * 2048;
    *(LAS v4u*)(uintptr_t)(scr + n16 * 64 + g4 * 16) = v0;
    *(LAS v4u*)(uintptr_t)(scr + 1024 + n16 * 64 + g4 * 16) = v1;
    const f32x4 z4 = 0.f;
    const f32x4 sa = mm16(asb(k0), asb(qf), z4), sb = mm16(asb(k1), asb(qf), z4);
    float sv[8], mx = -3.0e38f;
#pragma unroll
    for (int e = 0; e < 8; ++e) {
      const int dl = n16 + 16 - (16 * (e >> 2) + 4 * g4 + (e & 3));
      const bool okm = dl >= 0 && dl <= 14 && !(dl & 1) && tq - dl >= 0;
      sv[e] = okm ? (e < 4 ? sa[e & 3] : sb[e & 3]) * (0.1889822365046136f * 1.4426950408889634f) : -3.0e38f;
      mx = fmaxf(mx, sv[e]);
    }
    mx = fmaxf(mx, __shfl_xor(mx, 16));
    mx = fmaxf(mx, __shfl_xor(mx, 32));
    float sum = 0.f;
#pragma unroll
    for (int e = 0; e < 8; ++e) { sv[e] = __builtin_amdgcn_exp2f(sv[e] - mx); sum += sv[e]; }
    sum += __shfl_xor(sum, 16);
    sum += __shfl_xor(sum, 32);
    const float inv = __builtin_amdgcn_rcpf(sum);
    v4u pf;
#pragma unroll
    for (int e = 0; e < 4; ++e) pf[e] = pk(sv[2 * e] * inv, sv[2 * e + 1] * inv);
    f32x4 o[2];
#pragma unroll
    for (int t = 0; t < 2; ++t) {
      const unsigned a = scr + (4 * g4 + q4) * 64 + (((2 * p4 + t + odd) * 8) & 63);
      const v4s lo = __builtin_amdgcn_ds_read_tr16_b64_v4i16((LAS v4s*)(uintptr_t)a);
      const v4s hi = __builtin_amdgcn_ds_read_tr16_b64_v4i16((LAS v4s*)(uintptr_t)(a + 1024));
      o[t] = mm16(__builtin_shufflevector(lo, hi, 0, 1, 2, 3, 4, 5, 6, 7), asb(pf), z4);
    }
    v4u ov = {pk(o[0][0], o[0][1]), pk(o[0][2], o[0][3]), pk(o[1][0], o[1][1]), pk(o[1][2], o[1][3])};
    if (g4 == 3) { ov[2] = 0; ov[3] = 0; }
    __builtin_amdgcn_raw_buffer_store_b128(ov, rs, (tq >> 5) * HO_T + (tq & 31) * 16 + g4 * 512 + WS_HO + (224 + 4 * j) * 512, 0, AUX_SC1);
  }
  WAITL0();
  STAMPN(6);
  if (p2) {
    sfor<4>([&](auto i) { issueW(IC<9 + i>{}); });
    waitv<8>();
  } else waitv<0>();
  BAR();
  if (tid == 0) __hip_atomic_store((GAS u64*)(ws + WS_F2) + p * 16 + j, tag, __ATOMIC_RELAXED, __HIP_MEMORY_SCOPE_AGENT);
  STAMPN(7);
  if (!p2) {
    if (b == 255 && w == 0) {
#pragma unroll 1
      for (int k = 0; k < 4; ++k) poll(ws, WS_F1 + (k * 64 + lane) * 8, tag);
      if (lane == 0) __hip_atomic_store((GAS u64*)ws, tag, __ATOMIC_RELAXED, __HIP_MEMORY_SCOPE_AGENT);
    }
    return;
  }

  {
    const unsigned vA = vl + 2 * rg2 * HO_T + ksA * 1024, vB = vl + 2 * rg2 * HO_T + ksB * 1024;
    const unsigned fo2 = WS_F2 + ((rg2 >> 1) * 16 + n16) * 8;
    const unsigned vo = (8 * w + l3) * 896 + l7 * 16;
    const unsigned so = 64 * rg2 * 896 + cg * 128;
    v4u af[19][2], xres;
    f32x16 c2[2][2];
#pragma unroll
    for (int t = 0; t < 4; ++t) c2[t >> 1][t & 1] = 0.f;
    auto ldA = [&](auto i_) {
      constexpr int i = i_;
      ld16<WS_HO + i * 1024>(af[i][0], i < 8 ? vA : vB, rs);
      ld16<WS_HO + HO_T + i * 1024>(af[i][1], i < 8 ? vA : vB, rs);
    };
    auto step = [&](int i) {
      bf16x8 wa, wb;
      if (i < 9) { wa = w2[i][0]; wb = w2[i][1]; }
      else { wa = ldtr(a2 + wunit(i)); wb = ldtr((a2 ^ 64) + wunit(i)); }
      TIE(af[i][0]); TIE(af[i][1]);
      c2[0][0] = mm32(wa, asb(af[i][0]), c2[0][0]);
      c2[0][1] = mm32(wa, asb(af[i][1]), c2[0][1]);
      c2[1][0] = mm32(wb, asb(af[i][0]), c2[1][0]);
      c2[1][1] = mm32(wb, asb(af[i][1]), c2[1][1]);
      SCHED();
    };
    ld16<0>(xres, vo + so, rx);
    if (!owave) {
      sfor<8>([&](auto i) { ldA(i); });
      ld8(fl, fo2, rs);
      issueW(IC<13>{}); waitv<14 + 1 + 2>(); step(0);
      issueW(IC<14>{}); waitv<12 + 1 + 4>(); step(1);
      issueW(IC<15>{}); waitv<10 + 1 + 6>(); step(2);
      issueW(IC<16>{}); waitv<8 + 1 + 8>(); step(3);
      issueW(IC<17>{});
      if (nk > 18) issueW(IC<18>{});
      waitv<10>();
      TIE(fl);
      if (!__all(fl == tag)) poll(ws, fo2, tag);
      STAMPN(8);
      ldA(IC<8>{}); step(4);
      ldA(IC<9>{}); step(5);
      ldA(IC<10>{}); step(6);
      ldA(IC<11>{}); step(7);
      sfor<6>([&](auto i) { ldA(IC<12 + i>{}); });
      if (nk > 18) ldA(IC<18>{});
      sfor<10>([&](auto i) { waitv<2 * (9 - i)>(); step(8 + i); });
      if (nk > 18) step(18);
    } else {
      issueW(IC<13>{}); issueW(IC<14>{}); issueW(IC<15>{});
      poll(ws, fo2, tag);
      STAMPN(8);
      sfor<10>([&](auto i) { ldA(i); });
      sfor<16>([&](auto i_) {
        constexpr int i = i_;
        waitv<2 * (i < 6 ? 9 : 15 - i)>();
        step(i);
        if constexpr (i < 6) ldA(IC<10 + i>{});
      });
    }
    STAMPN(9);
#pragma unroll
    for (int nt = 0; nt < 2; ++nt)
#pragma unroll
      for (int tt = 0; tt < 2; ++tt)
#pragma unroll
        for (int c = 0; c < 2; ++c) {
          v4u o;
#pragma unroll
          for (int e = 0; e < 4; ++e) o[e] = pk(c2[nt][tt][8 * c + 2 * e], c2[nt][tt][8 * c + 2 * e + 1]);
          *(LAS v4u*)(uintptr_t)((2 * nt + c) * UNIT + w * 2048 + hh * 1024 + (((32 * tt + tok) ^ (4 * (2 * c + hh))) * 16)) = o;
        }
    WAITL0();
    BAR();
    STAMP(10);
    float f[8], sv[8];
#pragma unroll
    for (int e = 0; e < 8; ++e) { f[e] = 0.f; sv[e] = 0.f; }
    const unsigned ar = (l7 >> 1) * UNIT + (l7 & 1) * 1024 + (((8 * w + l3) ^ (4 * (l7 & 3))) * 16);
#pragma unroll
    for (int k = 0; k < 8; ++k) {
      const v4u v = *(const LAS v4u*)(uintptr_t)(ar + k * 2048);
#pragma unroll
      for (int e = 0; e < 4; ++e) {
        if (k < 6) { f[2 * e] += blo(v[e]); f[2 * e + 1] += bhi(v[e]); }
        else { sv[2 * e] += blo(v[e]); sv[2 * e + 1] += bhi(v[e]); }
      }
    }
    TIE(xres);
    float r[8];
#pragma unroll
    for (int e = 0; e < 8; ++e) {
      const float sg = f[e] * __builtin_amdgcn_rcpf(1.0f + __builtin_amdgcn_exp2f(f[e] * -1.4426950408889634f));
      r[e] = fmaf(sv[e], sg, (e & 1) ? bhi(xres[e >> 1]) : blo(xres[e >> 1]));
    }
    const v4u o = {pk(r[0], r[1]), pk(r[2], r[3]), pk(r[4], r[5]), pk(r[6], r[7])};
    *(GAS v4u*)(out + so + vo) = o;
    STAMP(11); CSTAMP(15);
  }
}

extern "C" void kernel_launch(void* const* d_in, const int* in_sizes, int n_in, void* d_out, int out_size, void* d_ws, size_t ws_size,
                              hipStream_t stream) {
  (void)in_sizes; (void)n_in; (void)out_size; (void)ws_size;
  (void)hipFuncSetAttribute((const void*)fused_block, hipFuncAttributeMaxDynamicSharedMemorySize, 163840);
  fused_block<<<256, 512, 163840, stream>>>((const char*)d_in[0], (const char*)d_in[1], (const char*)d_in[2], (const char*)d_in[4],
                                            (const char*)d_in[5], (const char*)d_in[6], (char*)d_out, (char*)d_ws);
}
```

```cpp
#include <hip/hip_runtime.h>
#include <stdint.h>
#include <stdio.h>
#include <utility>

#define GAS __attribute__((address_space(1)))
#define LAS __attribute__((address_space(3)))
typedef unsigned v4u __attribute__((ext_vector_type(4)));
typedef short v4s __attribute__((ext_vector_type(4)));
typedef short bf16x8 __attribute__((ext_vector_type(8)));
typedef float f32x4 __attribute__((ext_vector_type(4)));
typedef float f32x16 __attribute__((ext_vector_type(16)));
typedef float v2f __attribute__((ext_vector_type(2)));
typedef __bf16 v2bf __attribute__((ext_vector_type(2)));
typedef unsigned long long u64;

namespace {
constexpr unsigned FSTR = 4224;
constexpr unsigned WS_F1 = 65536, WS_F2 = WS_F1 + 16 * FSTR, WS_F3 = WS_F2 + 16 * FSTR;
constexpr unsigned WS_ST = 32768;
constexpr unsigned WS_QKV = 327680, QKV_T = 168 * 512;
constexpr unsigned WS_HO = WS_QKV + 64 * QKV_T, HO_T = 288 * 512;
constexpr unsigned WS_END = WS_HO + 64 * HO_T;
constexpr unsigned WS_DBG = 32u << 20;
constexpr u64 TAG_STEP = 0x9E3779B97F4A7C15ull;
constexpr int AUX_SC1 = 16;
constexpr unsigned SLOT = 40960, UNIT = 16384;
constexpr unsigned L_CSA = 122880, L_CSB = L_CSA + 3072, L_CNT = L_CSB + 2048;
constexpr unsigned CODE_BYTES = 20480;
constexpr unsigned OOB = 0x40000000u;
constexpr unsigned bunit(int s) { return (s == 0 ? 8 : s == 1 ? 9 : s == 2 ? 0 : s == 3 ? 1 : s == 4 ? 3 : s == 5 ? 4 : 5) * UNIT; }
constexpr unsigned wunit(int i) {
  return i == 18 ? 7 * UNIT : (i % 9) == 0 ? 2 * UNIT : (i % 9) == 1 ? 6 * UNIT : bunit(i % 9 - 2);
}
constexpr int PRO = 10;
constexpr int G_ST = 19, G_POLL = 50, G_ATT = 54, G_F1 = 42;
constexpr int avail(int g) {
  return g < 2 ? 20 : g < 6 ? 25 : g < 10 ? 30 : g < 14 ? 35 : g < 18 ? 39 : g < 22 ? 43 : g < 26 ? 49 : 53 + 2 * ((g - 26) / 4);
}
constexpr int cum(int g) {
  int c = PRO;
  for (int k = 0; k <= g; ++k) {
    int n = c + (k < 28 ? ((k & 1) && k != 27 ? 1 : 2) : 1);
    c = n < avail(k) ? n : avail(k);
  }
  return c;
}
static_assert(cum(55) == 67 && cum(27) == 53, "every item issued; B6 and W1 in front of the first store");
constexpr int rew(int g) { return g == 36 || g == 37 ? 9 : g == 40 || g == 41 ? 10 : g == 48 || g == 49 ? 11 : -1; }
constexpr int rreg(int g) { return g == 33 ? 0 : g == 37 ? 1 : g == 45 ? 2 : g == 49 ? 3 : -1; }
constexpr int nst(int g) { return g == 28 || g == 29 || g == 31 || g == 33 ? 1 : 0; }
constexpr int nextra(int g) {
  return (g == G_ST) + (rew(g) >= 0) + (g == G_ATT ? 3 : g == G_ATT + 1 ? 2 : 0);
}
constexpr int nops(int g) {
  int c = PRO + 3;
  for (int k = 0; k <= g; ++k) c += nst(k) + cum(k) - cum(k - 1) + nextra(k);
  return c;
}
constexpr int rankI(int it) {
  if (it < PRO) return it < 5 ? it + 1 : it + 2;
  int g = 0;
  while (cum(g) <= it) ++g;
  return nops(g - 1) + nst(g) + it - cum(g - 1);
}
constexpr int imin(int a, int b) { return a < b ? a : b; }
constexpr unsigned wbase(int g) {
  return g < 28 ? (((g >> 2) & 2) && g >= 0 ? 2 * SLOT + 16384 : 16384) : g < 36 ? 8 * UNIT : g < 44 ? 0 : 3 * UNIT;
}
constexpr bool isW(int it) { return it == 47 || it == 48 || it >= 51; }

template <int N> using IC = std::integral_constant<int, N>;
template <int... I, class F> __device__ __forceinline__ void sfor_(std::integer_sequence<int, I...>, F&& f) { (f(std::integral_constant<int, I>{}), ...); }
template <int N, class F> __device__ __forceinline__ void sfor(F&& f) { sfor_(std::make_integer_sequence<int, N>{}, f); }

#define WAITL0() asm volatile("s_waitcnt lgkmcnt(0)" ::: "memory")
#define BAR() do { asm volatile("" ::: "memory"); __builtin_amdgcn_s_barrier(); asm volatile("" ::: "memory"); } while (0)
#define SCHED() __builtin_amdgcn_sched_barrier(0)
#define PRIO(n) __builtin_amdgcn_s_setprio(n)
template <class T> __device__ __forceinline__ void TIE(T& v) { asm volatile("" : "+v"(v)); }
template <int N> __device__ __forceinline__ void waitv() { asm volatile("s_waitcnt vmcnt(%0)" ::"i"(N) : "memory"); }

typedef __amdgpu_buffer_rsrc_t rsrc_t;
__device__ __forceinline__ rsrc_t mkrs(const char* p, unsigned bytes) { return __builtin_amdgcn_make_buffer_rsrc((void*)p, 0, bytes, 0x00020000); }
template <unsigned L, unsigned S> __device__ __forceinline__ void dmac(unsigned voff, rsrc_t rs, unsigned ldsw) {
  unsigned t;
  asm volatile("s_add_u32 m0, %3, %4\n\ts_mov_b32 %0, %5\n\tbuffer_load_dwordx4 %1, %2, %0 offen lds"
               : "=&s"(t) : "v"(voff), "s"(rs), "s"(ldsw), "i"(L), "i"(S) : "memory", "scc");
}
template <unsigned L, unsigned S> __device__ __forceinline__ void dmam(unsigned voff, rsrc_t rs, unsigned ldsw, unsigned pitch) {
  unsigned t;
  asm volatile("s_add_u32 m0, %3, %4\n\ts_mul_i32 %0, %6, %5\n\tbuffer_load_dwordx4 %1, %2, %0 offen lds"
               : "=&s"(t) : "v"(voff), "s"(rs), "s"(ldsw), "i"(L), "i"(S), "s"(pitch) : "memory", "scc");
}
template <unsigned L, unsigned S, unsigned A> __device__ __forceinline__ void dmaa(unsigned voff, rsrc_t rs, unsigned ldsw, unsigned pitch) {
  unsigned t;
  asm volatile("s_add_u32 m0, %3, %4\n\ts_mul_i32 %0, %6, %5\n\ts_add_u32 %0, %0, %7\n\tbuffer_load_dwordx4 %1, %2, %0 offen lds"
               : "=&s"(t) : "v"(voff), "s"(rs), "s"(ldsw), "i"(L), "i"(S), "s"(pitch), "i"(A) : "memory", "scc");
}
template <unsigned S> __device__ __forceinline__ void ld16(v4u& d, unsigned voff, rsrc_t rs) {
  unsigned t;
  asm volatile("s_mov_b32 %1, %4\n\tbuffer_load_dwordx4 %0, %2, %3, %1 offen sc1" : "=v"(d), "=&s"(t) : "v"(voff), "s"(rs), "i"(S) : "memory");
}
template <unsigned S0, unsigned S1> __device__ __forceinline__ void ld16x2c(v4u& d0, v4u& d1, unsigned voff, rsrc_t rs, unsigned c) {
  unsigned t;
  asm volatile("s_cmp_eq_u32 %7, 0\n\ts_cbranch_scc1 1f\n\ts_mov_b32 %2, %5\n\tbuffer_load_dwordx4 %0, %3, %4, %2 offen sc1\n\t"
               "s_mov_b32 %2, %6\n\tbuffer_load_dwordx4 %1, %3, %4, %2 offen sc1\n1:"
               : "=&v"(d0), "=&v"(d1), "=&s"(t) : "v"(voff), "s"(rs), "i"(S0), "i"(S1), "s"(c) : "memory", "scc");
}
template <unsigned L, unsigned S, unsigned A> __device__ __forceinline__ void dmaac(unsigned voff, rsrc_t rs, unsigned ldsw, unsigned pitch, unsigned c) {
  unsigned t;
  asm volatile("s_cmp_eq_u32 %8, 0\n\ts_cbranch_scc1 1f\n\ts_add_u32 m0, %3, %4\n\ts_mul_i32 %0, %6, %5\n\ts_add_u32 %0, %0, %7\n\t"
               "buffer_load_dwordx4 %1, %2, %0 offen lds\n1:"
               : "=&s"(t) : "v"(voff), "s"(rs), "s"(ldsw), "i"(L), "i"(S), "s"(pitch), "i"(A), "s"(c) : "memory", "scc");
}
template <int N1, int N0> __device__ __forceinline__ void waitc(unsigned c) {
  static_assert(N0 <= N1 && N0 >= 0, "");
  asm volatile("s_cmp_lg_u32 %0, 0\n\ts_cbranch_scc1 1f\n\ts_waitcnt vmcnt(%2)\n1:\n\ts_waitcnt vmcnt(%1)" ::"s"(c), "i"(N1), "i"(N0) : "memory", "scc");
}
__device__ __forceinline__ void ld8(u64& d, unsigned voff, rsrc_t rs) {
  asm volatile("buffer_load_dwordx2 %0, %1, %2, 0 offen sc1" : "=v"(d) : "v"(voff), "s"(rs) : "memory");
}
__device__ __forceinline__ float wave_sum63(float v) {
#define DPP_ADD(ctrl, rm) v += __builtin_bit_cast(float, __builtin_amdgcn_update_dpp(0, __builtin_bit_cast(int, v), ctrl, rm, 0xf, true));
  DPP_ADD(0x111, 0xf) DPP_ADD(0x112, 0xf) DPP_ADD(0x114, 0xf) DPP_ADD(0x118, 0xf) DPP_ADD(0x142, 0xa) DPP_ADD(0x143, 0xc)
#undef DPP_ADD
  return v;
}
__device__ __forceinline__ bf16x8 ldtr(unsigned a) {
  v4s lo = __builtin_amdgcn_ds_read_tr16_b64_v4i16((LAS v4s*)(uintptr_t)a);
  v4s hi = __builtin_amdgcn_ds_read_tr16_b64_v4i16((LAS v4s*)(uintptr_t)(a + 512));
  return __builtin_shufflevector(lo, hi, 0, 1, 2, 3, 4, 5, 6, 7);
}
__device__ __forceinline__ unsigned pk(float a, float b) {
  v2f f = {a, b};
  return __builtin_bit_cast(unsigned, __builtin_convertvector(f, v2bf));
}
__device__ __forceinline__ float asf(unsigned d) { return __builtin_bit_cast(float, d); }
__device__ __forceinline__ float blo(unsigned d) { return __builtin_bit_cast(float, d << 16); }
__device__ __forceinline__ float bhi(unsigned d) { return __builtin_bit_cast(float, d & 0xffff0000u); }
__device__ __forceinline__ f32x16 mm32(bf16x8 a, bf16x8 b, f32x16 c) { return __builtin_amdgcn_mfma_f32_32x32x16_bf16(a, b, c, 0, 0, 0); }
__device__ __forceinline__ f32x4 mm16(bf16x8 a, bf16x8 b, f32x4 c) { return __builtin_amdgcn_mfma_f32_16x16x32_bf16(a, b, c, 0, 0, 0); }
__device__ __forceinline__ bf16x8 asb(v4u v) { return __builtin_bit_cast(bf16x8, v); }

__device__ __forceinline__ unsigned asu(float f) { return __builtin_bit_cast(unsigned, f); }
template <bool MAX> __device__ __forceinline__ float rows4(float v) {
  {
    const auto r = __builtin_amdgcn_permlane16_swap(asu(v), asu(v), false, false);
    const unsigned a = r[0], b = r[1];
    v = MAX ? fmaxf(asf(a), asf(b)) : asf(a) + asf(b);
  }
  const auto r = __builtin_amdgcn_permlane32_swap(asu(v), asu(v), false, false);
  const unsigned a = r[0], b = r[1];
  return MAX ? fmaxf(asf(a), asf(b)) : asf(a) + asf(b);
}
__device__ __forceinline__ void arrive(unsigned cnt, unsigned foff, rsrc_t rs, u64 tag) {
  unsigned a = cnt, t;
  u64 tv = tag;
  asm volatile("s_mov_b64 exec, 1\n\tv_mov_b32 %0, 1\n\tds_add_rtn_u32 %0, %1, %0\n\ts_waitcnt lgkmcnt(0)\n\t"
               "v_cmp_eq_u32 vcc, 7, %0\n\ts_cbranch_vccz 1f\n\tbuffer_store_dwordx2 %2, off, %3, %4 sc1\n1:\n\ts_mov_b64 exec, -1"
               : "=&v"(t) : "v"(a), "v"(tv), "s"(rs), "s"(foff) : "memory", "vcc");
}
__device__ __forceinline__ void ld8c(u64& d, unsigned voff, rsrc_t rs, unsigned c) {
  asm volatile("s_cmp_eq_u32 %3, 0\n\ts_cbranch_scc1 1f\n\tbuffer_load_dwordx2 %0, %1, %2, 0 offen sc1\n1:" : "=&v"(d) : "v"(voff), "s"(rs), "s"(c) : "memory", "scc");
}
__device__ __forceinline__ void poll(u64 v, unsigned voff, rsrc_t rs, u64 tag, unsigned c) {
  unsigned n = 1u << 19;
  asm volatile("s_cmp_eq_u32 %5, 0\n\ts_cbranch_scc1 2f\n\ts_waitcnt vmcnt(0)\n1:\n\tv_cmp_ne_u64 vcc, %4, %0\n\ts_cbranch_vccz 2f\n\ts_sleep 3\n\t"
               "buffer_load_dwordx2 %0, %2, %3, 0 offen sc1\n\ts_sub_u32 %1, %1, 1\n\ts_waitcnt vmcnt(0)\n\ts_cbranch_scc0 1b\n2:"
               : "+v"(v), "+s"(n) : "v"(voff), "s"(rs), "s"(tag), "s"(c) : "memory", "vcc", "scc");
}
__device__ __forceinline__ void waitw(unsigned lds) {
  unsigned a = lds, t, n = 1u << 22;
  asm volatile("1:\n\tds_read_b32 %0, %2\n\ts_waitcnt lgkmcnt(0)\n\tv_cmp_eq_u32 vcc, 0, %0\n\ts_cbranch_vccz 2f\n\ts_sleep 1\n\t"
               "s_sub_u32 %1, %1, 1\n\ts_cbranch_scc0 1b\n2:"
               : "=&v"(t), "+s"(n) : "v"(a) : "memory", "vcc", "scc");
}
__device__ __forceinline__ void pollg(v4u& v, unsigned voff, rsrc_t rs, u64 tag) {
  unsigned n = 1u << 20, v1 = v[1], v3 = v[3], t;
  const unsigned lo = (unsigned)tag, hi = (unsigned)(tag >> 32);
  if (__all(v1 == lo && v3 == hi)) return;
#pragma unroll 1
  for (; n; --n) {
    v = __builtin_amdgcn_raw_buffer_load_b128(rs, voff, 0, AUX_SC1);
    v1 = v[1]; v3 = v[3];
    if (__all(v1 == lo && v3 == hi)) break;
    __builtin_amdgcn_s_sleep(1);
  }
  (void)t;
}
__device__ __forceinline__ bf16x8 csfrag(unsigned lds) {
  const f32x4 p = *(const LAS f32x4*)(uintptr_t)lds;
  const float c = (p[0] + p[1]) + (p[2] + p[3]);
  const unsigned h = pk(c, 0.f);
  const unsigned d = pk(c, c - blo(h));
  return asb(v4u{d, d, 0u, 0u});
}
template <int C> __device__ __forceinline__ void store_q(const f32x16& a, float rstd, rsrc_t rs, unsigned off) {
  v4u o;
#pragma unroll
  for (int e = 0; e < 4; ++e) o[e] = pk(a[8 * C + 2 * e] * rstd, a[8 * C + 2 * e + 1] * rstd);
  __builtin_amdgcn_raw_buffer_store_b128(o, rs, off + C * 1024, 0, AUX_SC1);
}
template <int Q> __device__ __forceinline__ void swiglu4(const f32x16& g, const f32x16& u, float rstd, v4u& o) {
  const float k1 = -1.4426950408889634f * rstd, k2 = rstd * rstd;
  float h[4];
#pragma unroll
  for (int e = 0; e < 4; ++e) {
    const float gg = g[4 * Q + e];
    const float r = __builtin_amdgcn_rcpf(1.0f + __builtin_amdgcn_exp2f(gg * k1));
    h[e] = (gg * k2) * (u[4 * Q + e] * r);
  }
  o[2 * (Q & 1)] = pk(h[0], h[1]);
  o[2 * (Q & 1) + 1] = pk(h[2], h[3]);
}
#define STAMP(i)
#define CSTAMP(i)
#define STAMPB(i)
#define STAMPN(i) STAMP(i)
}

__global__ __launch_bounds__(512, 2) void fused_block(const char* __restrict__ x, const char* __restrict__ wqkv, const char* __restrict__ wo,
                                                      const char* __restrict__ wg, const char* __restrict__ wu, const char* __restrict__ wd,
                                                      char* __restrict__ out, char* __restrict__ ws) {
  STAMP(0);
  asm volatile("" ::"s"(x), "s"(wqkv), "s"(wo), "s"(wg), "s"(wu), "s"(wd), "s"(out), "s"(ws));
  const int tid = threadIdx.x, lane = tid & 63, w = __builtin_amdgcn_readfirstlane(tid >> 6);
  const int rg = w & 3, set = w >> 2, rr = rg ^ (2 * set);
  const int b = blockIdx.x, xl = b & 7, idx = b >> 3;
  const int j = (idx & 3) < 3 ? 3 * (xl & 3) + (idx & 3) : 12 + (xl & 3), p = 8 * (xl >> 2) + (idx >> 2);
  const bool typeA = j < 12;
  const bool p2 = idx < 28;
  const int tok = lane & 31, hh = lane >> 5;
  const unsigned lw1 = w * 1024, lw2 = w * 2048;
  const rsrc_t rs = mkrs(ws, WS_END), rx = mkrs(x, 2048 * 896);

  const int ts = 128 * p + 8 * j + w;
  v4u xrow;
  unsigned warm;
  asm volatile("buffer_load_dwordx4 %0, %1, %2, 0 offen" : "=v"(xrow) : "v"(lane < 56 ? ts * 896 + lane * 16 : OOB), "s"(rx) : "memory");
  const unsigned l3 = lane >> 3, l7 = lane & 7;
  const unsigned cw = (l7 ^ (((lane >> 4) & 1) << 2)) * 16;
  const unsigned vx0 = (128 * p + 16 * w + l3) * 896 + ((l7 ^ (lane >> 4)) * 16), vx1 = (vx0 ^ 64) + 8 * 896;
  const int qb0 = typeA ? j : (j == 15 ? 18 : 3 * j - 24);
  const unsigned vq = (8 * w + l3) * 2688 + qb0 * 128 + cw, vf = (8 * w + l3) * 3584 + j * 128 + cw, v12 = typeA ? vf : vq;
  const rsrc_t rq = mkrs(wqkv, 448 * 2688), rgt = mkrs(wg, 448 * 3584), rup = mkrs(wu, 448 * 3584);
  const rsrc_t r1 = mkrs(typeA ? wg : wqkv + 128, typeA ? 448 * 3584 : 448 * 2688 - 128);
  const rsrc_t r2 = mkrs(typeA ? wu : wqkv + 256, typeA ? 448 * 3584 : 448 * 2688 - 256);
  const unsigned st12 = typeA ? 64 * 3584 : 64 * 2688;
  auto itemA = [&](auto it_) {
    constexpr int it = it_, s = it / 5, k = it % 5;
    constexpr unsigned sl = (s & 3) * SLOT;
    if constexpr (k == 0) dmac<sl, s * 128>(vx0, rx, lw2);
    else if constexpr (k == 1) dmac<sl + 1024, s * 128>(vx1, rx, lw2);
    else if constexpr (k == 2) dmac<sl + 16384, s * 64 * 2688>(vq, rq, lw1);
    else if constexpr (k == 3) dmam<sl + 24576, s>(v12, r1, lw1, st12);
    else dmam<sl + 32768, s>(v12, r2, lw1, st12);
  };
  sfor<5>([&](auto it) { itemA(it); });
  {
    u64 pc;
    asm volatile("s_getpc_b64 %0" : "=s"(pc));
    const unsigned o = ((idx * 8 + w) * 8 + (lane & 7)) * 128 % CODE_BYTES;
    asm volatile("global_load_dword %0, %1, %2" : "=v"(warm) : "v"(o), "s"(pc) : "memory");
  }
  sfor<PRO - 5>([&](auto it) { itemA(IC<5 + it>{}); });
  SCHED();
  const int rg2 = p2 ? 4 * xl + idx / 7 : 2 * p, cg = idx % 7;
  const bool owave = w >= 6;
  const unsigned cO = (w + 2) & 8, cW = cO ^ 8, cH = __builtin_amdgcn_readfirstlane(p2 ? cW : 0u), c18 = (w & 4) ^ 4, cC = (j + 4) & 16, c7 = (w + 1) & 8;
  const int ksA = owave ? 122 + 16 * (w - 6) : 8 * w;
  const int ksB = owave ? 112 + 16 * (w - 6) : 48 + (w < 5 ? 11 * w : 54);
  const unsigned pstr = owave ? 28 * 896 : 32 * 896;
  const rsrc_t rw2 = mkrs(owave ? wo : wd, owave ? 448 * 896 : 1792 * 896);
  const unsigned vdA = p2 ? (owave ? (w - 6) * 224 + 140 : ksA * 16) * 896 + l3 * 896 + cg * 128 + cw : OOB;
  const unsigned vdB = p2 ? (owave ? (w - 6) * 224 : ksB * 16) * 896 + l3 * 896 + cg * 128 + cw : OOB;
  auto issueWh = [&](auto i_, auto hf_) {
    constexpr int i = i_, hf = hf_;
    constexpr unsigned L = wunit(i) + hf * 1024, S = (i & 7) >> 1 | (i >= 16 ? 4 : 0), A = ((i & 1) * 16 + hf * 8) * 896;
    if constexpr (i == 6 || i == 7) dmaac<L, S, A>(vdA, rw2, lw2, pstr, cW);
    else if constexpr (i == 18) dmaac<L, S, A>(vdB, rw2, lw2, pstr, c18);
    else dmaa<L, S, A>(i < 8 ? vdA : vdB, rw2, lw2, pstr);
  };
  auto issueW = [&](auto i) { issueWh(i, IC<0>{}); issueWh(i, IC<1>{}); };
  auto item = [&](auto it_) {
    constexpr int it = it_;
    if constexpr (it < 35) {
      itemA(it_);
    } else if constexpr (isW(it)) {
      issueWh(IC<(it < 49 ? 0 : (it - 49) >> 1)>{}, IC<((it - 47) & 1)>{});
    } else {
      constexpr int s = it < 47 ? (it - 35) >> 1 : 6, k = (it - 35) & 1;
      dmac<bunit(s) + k * 8192, s * 64 * 3584 + 12 * 128>(vf, k ? rup : rgt, lw1);
    }
  };
  u64 tag;
  asm volatile("s_load_dwordx2 %0, %1, 0x0\n\ts_waitcnt lgkmcnt(0)" : "=s"(tag) : "s"(ws) : "memory");
  tag += TAG_STEP;
  waitv<PRO + 1>();
  TIE(xrow);
  {
    float a = 0.f, q = 0.f;
#pragma unroll
    for (int e = 0; e < 4; ++e) {
      const float lo = blo(xrow[e]), hi = bhi(xrow[e]);
      a += lo + hi; q = fmaf(lo, lo, fmaf(hi, hi, q));
    }
    a = wave_sum63(a); q = wave_sum63(q);
    const float m = a * (1.0f / 448.0f);
    const float r = __builtin_amdgcn_rsqf(fmaxf(q * (1.0f / 448.0f) - m * m, 0.f) + 1e-5f);
    const v4u gr = {__builtin_bit_cast(unsigned, m), (unsigned)tag, __builtin_bit_cast(unsigned, r), (unsigned)(tag >> 32)};
    __builtin_amdgcn_raw_buffer_store_b128(gr, rs, lane == 63 ? WS_ST + ts * 16 : OOB, 0, AUX_SC1);
  }

  const unsigned q4 = (lane & 15) >> 2, p4 = lane & 3, sp4 = ((p4 & 1) << 1) | (p4 >> 1);
  const unsigned trl = (8 * hh + q4) * 128 + (4 * ((lane >> 4) & 1) + sp4) * 8;
  const unsigned aw = (trl + ((set ^ (q4 >> 1)) << 6)) | (rr << 11);
  const unsigned ax = (32 * rg + tok) * 128 + (((2 * rr + hh) ^ ((tok >> 1) & 7)) << 4);
  const unsigned selw = (((lane >> 4) ^ (lane >> 2)) & 1) ? 0u : 0x3F803F80u;
  const bf16x8 sel = asb(v4u{selw, selw, selw, selw});
  unsigned bw[4], bx[4];
#pragma unroll
  for (int i = 0; i < 4; ++i) { bw[i] = (aw ^ (i * 2048)) + 16384; bx[i] = ax ^ (i * 32); TIE(bw[i]); TIE(bx[i]); }

  bf16x8 xf[28];
  f32x16 acc[3], accB[2];
  f32x4 cs[3], csB[2];
#pragma unroll
  for (int t = 0; t < 3; ++t) { acc[t] = 0.f; cs[t] = 0.f; }
#pragma unroll
  for (int t = 0; t < 2; ++t) { accB[t] = 0.f; csB[t] = 0.f; }
  bf16x8 wf[3][3];
  auto rdX = [&](auto n_) {
    constexpr int n = n_, s = n >> 2, i = n & 3;
    if constexpr (s == 2 || s == 6) { bx[i] += 2 * SLOT; TIE(bx[i]); }
    if constexpr (s == 4) { bx[i] -= 2 * SLOT; TIE(bx[i]); }
    xf[n] = *(const LAS bf16x8*)(uintptr_t)(bx[i] + (s & 1) * SLOT);
  };
  auto rdW = [&](auto g_, auto t_) {
    constexpr int g = g_, t = t_, i = g & 3;
    if constexpr (t == 0 && wbase(g) != wbase(g - 4)) { bw[i] += wbase(g) - wbase(g - 4); TIE(bw[i]); }
    constexpr unsigned off = (g < 28 ? ((g >> 2) & 3) * SLOT + 16384 : bunit((g - 28) >> 2)) + t * 8192 - wbase(g);
    static_assert(off + 512 < 65536, "ds offset");
    wf[g % 3][t] = ldtr(bw[i] + off);
  };
  const unsigned tt1 = 4 * p + rg;
  const unsigned vl = lane * 16;
  v4u stg;
  float rstd;
  bf16x8 mu, cf[3];

  waitv<PRO - 5 + 1>();
  TIE(warm);
  BAR();
  STAMP(1); CSTAMP(12);
  sfor<2>([&](auto n) { rdX(n); rdW(n, IC<0>{}); rdW(n, IC<1>{}); rdW(n, IC<2>{}); });
  sfor<28>([&](auto n_) {
    constexpr int n = n_, s = n >> 2, i = n & 3, c0 = cum(n - 1), c1 = cum(n);
    constexpr int NT = n < 26 ? 3 : 2;
    SCHED();
    if constexpr (i != 2) PRIO(3 - ((i + 2) & 3));
    acc[0] = mm32(wf[n % 3][0], xf[n], acc[0]);
    SCHED();
    if constexpr (i == 2) {
      if constexpr (s == 4) { if (tid < 4) *(LAS unsigned*)(uintptr_t)(L_CNT + tid * 4) = 0u; }
      if constexpr (s == 6) {
#pragma unroll
        for (int t = 0; t < 3; ++t) *(LAS float*)(uintptr_t)(L_CSA + ((set * 3 + t) * 32 + tok) * 16 + rg * 4) = cs[t][0];
      }
      WAITL0();
      static_assert(c0 > (s < 6 ? 5 * s + 9 : 36), "next stage issued");
      constexpr int y = nops(n - 1) - 1 - rankI(s < 6 ? 5 * s + 9 : 36);
      waitv<s == 6 ? imin(y, nops(n - 1) - nops(G_ST)) : y>();
      BAR();
      PRIO(3);
    }
    if constexpr (n < 26) rdX(IC<n + 2>{});
    rdW(IC<n + 2>{}, IC<0>{});
    if constexpr (i == 1) { rdW(IC<n + 2>{}, IC<1>{}); rdW(IC<n + 2>{}, IC<2>{}); }
    if constexpr (n == 26) {
      TIE(stg);
      pollg(stg, WS_ST + (tt1 * 32 + tok) * 16, rs, tag);
      rstd = asf(stg[2]);
      const float m = asf(stg[0]);
      const unsigned mh = pk(-m, -m);
      const float ml = -m - blo(mh);
      mu = asb(v4u{hh ? 0u : mh, hh ? 0u : pk(ml, ml), 0u, 0u});
      TIE(mu);
    }
    if constexpr (n == 27) { cf[2] = csfrag(L_CSA + ((set * 3 + 2) * 32 + tok) * 16); TIE(cf[2]); }
    SCHED();
    if constexpr (i == 0) cs[0] = mm16(sel, wf[n % 3][0], cs[0]);
    if constexpr (n == 27) acc[0] = mm32(cf[0], mu, acc[0]);
    acc[1] = mm32(wf[n % 3][1], xf[n], acc[1]);
    SCHED();
    if constexpr (i != 1) rdW(IC<n + 2>{}, IC<1>{});
    if constexpr (c1 > c0) item(IC<c0>{});
    if constexpr (n == 26) { cf[0] = csfrag(L_CSA + ((set * 3 + 0) * 32 + tok) * 16); TIE(cf[0]); }
    SCHED();
    if constexpr (i == 0) cs[1] = mm16(sel, wf[n % 3][1], cs[1]);
    if constexpr (n == 27) acc[1] = mm32(cf[1], mu, acc[1]);
    acc[2] = mm32(wf[n % 3][2], xf[n], acc[2]);
    SCHED();
    if constexpr (i != 1 && NT == 3) rdW(IC<n + 2>{}, IC<2>{});
    if constexpr (c1 > c0 + 1) item(IC<c0 + 1>{});
    if constexpr (n == G_ST) ld16<WS_ST>(stg, (tt1 * 32 + tok) * 16, rs);
    if constexpr (n == 26) { cf[1] = csfrag(L_CSA + ((set * 3 + 1) * 32 + tok) * 16); TIE(cf[1]); }
    SCHED();
    if constexpr (i == 0) cs[2] = mm16(sel, wf[n % 3][2], cs[2]);
    if constexpr (n == 27) acc[2] = mm32(cf[2], mu, acc[2]);
  });
  SCHED();
  STAMP(2); CSTAMP(13);
  const unsigned oQ = j < 15 ? vl + WS_QKV + tt1 * QKV_T + (2 * qb0 + set) * 2048 : OOB;
  const unsigned oH = vl + WS_HO + tt1 * HO_T + (2 * j + set) * 2048;
  const int n16 = lane & 15, g4 = lane >> 4, odd = j & 1;
  const int tq = 128 * p + 16 * w + n16, tk = tq >= 16 ? tq - 16 : tq;
  const unsigned oq = (tq >> 5) * QKV_T + (tq & 31) * 16 + (g4 + ((7 * j) >> 1)) * 512;
  const unsigned ok = (tk >> 5) * QKV_T + (tk & 31) * 16 + (g4 + ((7 * j) >> 1)) * 512;
  const unsigned fo1 = WS_F1 + (g4 == 1 && p > 0 ? p - 1 : g4 == 2 ? rg2 >> 1 : p) * FSTR + n16 * 8;
  const unsigned a2 = w * 2048 + trl + ((q4 >> 1) << 6);
  u64 fl;
  v4u qf, k0, k1, v0, v1, hv;
  bf16x8 w2[9][2];
  auto toReg = [&](auto i) {
    w2[i][0] = ldtr(a2 + wunit(i)); w2[i][1] = ldtr((a2 ^ 64) + wunit(i));
  };
  sfor<28>([&](auto n_) {
    constexpr int n = n_, s = n >> 2, i = n & 3, g = 28 + n, c0 = cum(g - 1), c1 = cum(g);
    SCHED();
    if constexpr (i != 2) PRIO(3 - ((i + 2) & 3));
    accB[0] = mm32(wf[g % 3][0], xf[n], accB[0]);
    SCHED();
    if constexpr (i == 2) {
      if constexpr (s == 6) {
#pragma unroll
        for (int t = 0; t < 2; ++t) *(LAS float*)(uintptr_t)(L_CSB + ((set * 2 + t) * 32 + tok) * 16 + rg * 4) = csB[t][0];
      }
      WAITL0();
      if constexpr (g == G_F1) STAMPB(6);
      if constexpr (s == 0) waitv<nops(g - 1) - 1 - rankI(48)>();
      if constexpr (s == 1) {
        waitc<nops(g - 1) - 1 - rankI(52) + 2, nops(g - 1) - 1 - rankI(52)>(cC);
      }
      if constexpr (g == G_F1) waitv<nops(g - 1) - 1 - nops(32)>();
      if constexpr (g == 46) waitv<nops(g - 1) - 1 - rankI(56)>();
      if constexpr (g == G_ATT) { STAMPB(8); poll(fl, fo1, rs, tag, c7); }
      BAR();
      if constexpr (g == G_ATT) STAMPB(9);
      PRIO(3);
      if constexpr (s == 1) STAMPB(5);
      if constexpr (g == G_F1) {
        STAMPB(7);
        if (tid == 0) __hip_atomic_store((GAS u64*)(ws + (WS_F1 + p * FSTR + j * 8)), tag, __ATOMIC_RELAXED, __HIP_MEMORY_SCOPE_AGENT);
      }
    }
    if constexpr (n < 26) {
      rdW(IC<g + 2>{}, IC<0>{});
      if constexpr (i == 1) rdW(IC<g + 2>{}, IC<1>{});
    }
    if constexpr (rreg(g) >= 0) toReg(IC<rreg(g)>{});
    if constexpr (n == 0) store_q<0>(acc[0], rstd, rs, oQ);
    if constexpr (n == 1) store_q<1>(acc[0], rstd, rs, oQ);
    if constexpr (n >= 2 && n <= 5) {
      constexpr int Q = n - 2;
      if (typeA) {
        swiglu4<Q>(acc[1], acc[2], rstd, hv);
        if constexpr (Q & 1) __builtin_amdgcn_raw_buffer_store_b128(hv, rs, oH + (Q >> 1) * 1024, 0, AUX_SC1);
        else TIE(hv);
      } else store_q<(Q & 1)>(acc[1 + (Q >> 1)], rstd, rs, oQ + (1 + (Q >> 1)) * 4096);
    }
    if constexpr (n == 26) { cf[0] = csfrag(L_CSB + ((set * 2 + 0) * 32 + tok) * 16); TIE(cf[0]); }
    if constexpr (n == 27) { cf[1] = csfrag(L_CSB + ((set * 2 + 1) * 32 + tok) * 16); TIE(cf[1]); }
    SCHED();
    if constexpr (i == 0) csB[0] = mm16(sel, wf[g % 3][0], csB[0]);
    if constexpr (n == 27) accB[0] = mm32(cf[0], mu, accB[0]);
    accB[1] = mm32(wf[g % 3][1], xf[n], accB[1]);
    SCHED();
    if constexpr (n < 26 && i != 1) rdW(IC<g + 2>{}, IC<1>{});
    sfor<c1 - c0>([&](auto k) { item(IC<c0 + k>{}); });
    if constexpr (rew(g) >= 0) issueWh(IC<rew(g)>{}, IC<(g & 1)>{});
    if constexpr (g == G_ATT) {
      ld16<WS_QKV>(qf, oq, rs);
      ld16<WS_QKV + 56 * 512>(k0, ok, rs);
      ld16<WS_QKV + 56 * 512>(k1, oq, rs);
    }
    if constexpr (g == G_ATT + 1) { ld16<WS_QKV + 112 * 512>(v0, ok, rs); ld16<WS_QKV + 112 * 512>(v1, oq, rs); }
    if constexpr (g == G_POLL) ld8c(fl, fo1, rs, c7);
    SCHED();
    if constexpr (i == 0) csB[1] = mm16(sel, wf[g % 3][1], csB[1]);
    if constexpr (n == 27) accB[1] = mm32(cf[1], mu, accB[1]);
  });
  SCHED();
  PRIO(0);
  STAMP(4); CSTAMP(14);
  const unsigned vA = vl + 2 * rg2 * HO_T + ksA * 1024, vB = vl + 2 * rg2 * HO_T + ksB * 1024;
  v4u af[19][2];
  auto ldE = [&](auto i_, unsigned c) {
    constexpr int i = i_;
    ld16x2c<WS_HO + i * 1024, WS_HO + HO_T + i * 1024>(af[i][0], af[i][1], vA, rs, c);
  };
  auto ldL = [&](auto i_) {
    constexpr int i = i_;
    if constexpr (i == 18) ld16x2c<WS_HO + (i - 8) * 1024, WS_HO + HO_T + (i - 8) * 1024>(af[i][0], af[i][1], vB, rs, c18);
    else {
      ld16<WS_HO + (i - 8) * 1024>(af[i][0], vB, rs);
      ld16<WS_HO + HO_T + (i - 8) * 1024>(af[i][1], vB, rs);
    }
  };
  swiglu4<0>(accB[0], accB[1], rstd, hv); swiglu4<1>(accB[0], accB[1], rstd, hv);
  __builtin_amdgcn_raw_buffer_store_b128(hv, rs, oH + 24 * 2048, 0, AUX_SC1);
  swiglu4<2>(accB[0], accB[1], rstd, hv); swiglu4<3>(accB[0], accB[1], rstd, hv);
  __builtin_amdgcn_raw_buffer_store_b128(hv, rs, oH + 24 * 2048 + 1024, 0, AUX_SC1);
  SCHED();
  STAMPN(5);
  sfor<3>([&](auto i) { ldE(i, cH); });
  waitc<2 + 6, 2>(cH);
  TIE(qf); TIE(k0); TIE(k1); TIE(v0); TIE(v1);
  {
    if (odd && g4 == 0) { qf[0] = 0; qf[1] = 0; }
    if (!odd && g4 == 3) { qf[2] = 0; qf[3] = 0; }
    const unsigned scr = wunit(3) + w * 2048;
    *(LAS v4u*)(uintptr_t)(scr + n16 * 64 + g4 * 16) = v0;
    *(LAS v4u*)(uintptr_t)(scr + 1024 + n16 * 64 + g4 * 16) = v1;
    const f32x4 z4 = 0.f;
    const f32x4 sa = mm16(asb(k0), asb(qf), z4), sb = mm16(asb(k1), asb(qf), z4);
    bf16x8 vt[2];
#pragma unroll
    for (int t = 0; t < 2; ++t) {
      const unsigned a = scr + (4 * g4 + q4) * 64 + (((2 * p4 + t + odd) * 8) & 63);
      const v4s lo = __builtin_amdgcn_ds_read_tr16_b64_v4i16((LAS v4s*)(uintptr_t)a);
      const v4s hi = __builtin_amdgcn_ds_read_tr16_b64_v4i16((LAS v4s*)(uintptr_t)(a + 1024));
      vt[t] = __builtin_shufflevector(lo, hi, 0, 1, 2, 3, 4, 5, 6, 7);
    }
    sfor<5>([&](auto i) { toReg(IC<4 + i>{}); });
    const bool par = n16 & 1, c0 = n16 - 4 * g4 >= 0, c1 = n16 - 4 * g4 >= 2, back = tq >= 16;
    const float ninf = -3.0e38f, sc = 0.1889822365046136f * 1.4426950408889634f;
    const float s0 = c0 ? (par ? sb[1] : sb[0]) * sc : back ? (par ? sa[1] : sa[0]) * sc : ninf;
    const float s1 = c1 ? (par ? sb[3] : sb[2]) * sc : back ? (par ? sa[3] : sa[2]) * sc : ninf;
    const float mx = rows4<true>(fmaxf(s0, s1));
    const float e0 = __builtin_amdgcn_exp2f(s0 - mx), e1 = __builtin_amdgcn_exp2f(s1 - mx);
    const float inv = __builtin_amdgcn_rcpf(rows4<false>(e0 + e1));
    const unsigned u0 = par ? pk(0.f, e0 * inv) : pk(e0 * inv, 0.f), u1 = par ? pk(0.f, e1 * inv) : pk(e1 * inv, 0.f);
    const v4u pf = {c0 ? 0u : u0, c1 ? 0u : u1, c0 ? u0 : 0u, c1 ? u1 : 0u};
    const f32x4 oa = mm16(vt[0], asb(pf), z4), ob = mm16(vt[1], asb(pf), z4);
    v4u ov = {pk(oa[0], oa[1]), pk(oa[2], oa[3]), pk(ob[0], ob[1]), pk(ob[2], ob[3])};
    if (g4 == 3) { ov[2] = 0; ov[3] = 0; }
    __builtin_amdgcn_raw_buffer_store_b128(ov, rs, (tq >> 5) * HO_T + (tq & 31) * 16 + g4 * 512 + WS_HO + (224 + 4 * j) * 512, 0, AUX_SC1);
  }
  SCHED();
  STAMPN(6);
  sfor<3>([&](auto i) { ldE(IC<3 + i>{}, cH); });
  WAITL0();
  issueW(IC<12>{});
  waitc<6 + 1 + 6 + 2, 1 + 2>(cH);
  arrive(L_CNT, WS_F2 + p * FSTR + j * 8, rs, tag);
  STAMPN(7);
  sfor<2>([&](auto i) { ldE(IC<6 + i>{}, cH); });
  issueW(IC<13>{});
  waitc<6 + 2 + 4 + 2, 2 + 2>(cH);
  arrive(L_CNT + 4, WS_F3 + p * FSTR + j * 8, rs, tag);
  if (!p2) {
    if (b == 255 && w == 0) {
#pragma unroll 1
      for (int k = 0; k < 4; ++k) poll(~tag, WS_F1 + (k * 4 + g4) * FSTR + n16 * 8, rs, tag, 1u);
      if (lane == 0) __hip_atomic_store((GAS u64*)ws, tag, __ATOMIC_RELAXED, __HIP_MEMORY_SCOPE_AGENT);
    }
    return;
  }

  {
    const unsigned fo2 = (w == 6 ? WS_F3 : WS_F2) + (rg2 >> 1) * FSTR + n16 * 8;
    const unsigned vo = (64 * rg2 + 8 * w + l3) * 896 + cg * 128 + l7 * 16;
    v4u xres;
    f32x16 c2[2][2];
#pragma unroll
    for (int t = 0; t < 4; ++t) c2[t >> 1][t & 1] = 0.f;
    auto step = [&](auto i_) {
      constexpr int i = i_;
      bf16x8 wa, wb;
      if constexpr (i < 9) { wa = w2[i][0]; wb = w2[i][1]; }
      else { wa = ldtr(a2 + wunit(i)); wb = ldtr((a2 ^ 64) + wunit(i)); }
      TIE(af[i][0]); TIE(af[i][1]);
      c2[0][0] = mm32(wa, asb(af[i][0]), c2[0][0]);
      c2[0][1] = mm32(wa, asb(af[i][1]), c2[0][1]);
      c2[1][0] = mm32(wb, asb(af[i][0]), c2[1][0]);
      c2[1][1] = mm32(wb, asb(af[i][1]), c2[1][1]);
      SCHED();
    };
    ld16<0>(xres, vo, rx);
    issueW(IC<14>{});
    if (!owave) { step(IC<0>{}); step(IC<1>{}); }
    issueW(IC<15>{});
    if (!owave) { step(IC<2>{}); waitv<2 + 4 + 2 + 1 + 4>(); step(IC<3>{}); }
    issueW(IC<16>{});
    if (!owave) { step(IC<4>{}); step(IC<5>{}); }
    issueW(IC<17>{});
    issueW(IC<18>{});
    if (!owave) { waitv<2 + 1 + 4 + 4>(); step(IC<6>{}); step(IC<7>{}); }
    poll(~tag, fo2, rs, tag, cO);
    if (owave) *(LAS unsigned*)(uintptr_t)(L_CNT + 8 + (w & 1) * 4) = 1u;
    waitw(L_CNT + (owave ? 8 : 12));
    STAMPN(8);
    sfor<6>([&](auto i) { ldE(i, cO); });
    sfor<4>([&](auto i) { ldL(IC<8 + i>{}); });
    sfor<6>([&](auto i) {
      if (owave) { waitv<18>(); step(i); }
      ldL(IC<12 + i>{});
    });
    ldL(IC<18>{});
    sfor<10>([&](auto i) { waitv<2 * (9 - i)>(); step(IC<8 + i>{}); });
    if (w < 4) step(IC<18>{});
    STAMPN(9);
#pragma unroll
    for (int nt = 0; nt < 2; ++nt)
#pragma unroll
      for (int tt = 0; tt < 2; ++tt)
#pragma unroll
        for (int c = 0; c < 2; ++c) {
          v4u o;
#pragma unroll
          for (int e = 0; e < 4; ++e) o[e] = pk(c2[nt][tt][8 * c + 2 * e], c2[nt][tt][8 * c + 2 * e + 1]);
          *(LAS v4u*)(uintptr_t)((2 * nt + c) * UNIT + w * 2048 + hh * 1024 + (((32 * tt + tok) ^ (4 * (2 * c + hh))) * 16)) = o;
        }
    WAITL0();
    BAR();
    STAMP(10);
    float f[8], sv[8];
#pragma unroll
    for (int e = 0; e < 8; ++e) { f[e] = 0.f; sv[e] = 0.f; }
    const unsigned ar = (l7 >> 1) * UNIT + (l7 & 1) * 1024 + (((8 * w + l3) ^ (4 * (l7 & 3))) * 16);
#pragma unroll
    for (int k = 0; k < 8; ++k) {
      const v4u v = *(const LAS v4u*)(uintptr_t)(ar + k * 2048);
#pragma unroll
      for (int e = 0; e < 4; ++e) {
        if (k < 6) { f[2 * e] += blo(v[e]); f[2 * e + 1] += bhi(v[e]); }
        else { sv[2 * e] += blo(v[e]); sv[2 * e + 1] += bhi(v[e]); }
      }
    }
    TIE(xres);
    float r[8];
#pragma unroll
    for (int e = 0; e < 8; ++e) {
      const float sg = f[e] * __builtin_amdgcn_rcpf(1.0f + __builtin_amdgcn_exp2f(f[e] * -1.4426950408889634f));
      r[e] = fmaf(sv[e], sg, (e & 1) ? bhi(xres[e >> 1]) : blo(xres[e >> 1]));
    }
    const v4u o = {pk(r[0], r[1]), pk(r[2], r[3]), pk(r[4], r[5]), pk(r[6], r[7])};
    *(GAS v4u*)(out + vo) = o;
    STAMP(11); CSTAMP(15);
  }
}

extern "C" void kernel_launch(void* const* d_in, const int* in_sizes, int n_in, void* d_out, int out_size, void* d_ws, size_t ws_size,
                              hipStream_t stream) {
  (void)in_sizes; (void)n_in; (void)out_size; (void)ws_size;
  for (int i = 0; i < 7; ++i) { hipPointerAttribute_t a; hipError_t e = hipPointerGetAttributes(&a, d_in[i]); fprintf(stderr, "in%d %p e%d type%d flags%u managed%d\n", i, d_in[i], (int)e, (int)a.type, a.allocationFlags, a.isManaged); }
  fprintf(stderr, "out %p ws %p\n", d_out, d_ws);
  (void)hipFuncSetAttribute((const void*)fused_block, hipFuncAttributeMaxDynamicSharedMemorySize, 163840);
  fused_block<<<256, 512, 163840, stream>>>((const char*)d_in[0], (const char*)d_in[1], (const char*)d_in[2], (const char*)d_in[4],
                                            (const char*)d_in[5], (const char*)d_in[6], (char*)d_out, (char*)d_ws);
}
```

```cpp
#include <hip/hip_runtime.h>
#include <stdint.h>
#include <utility>

#define GAS __attribute__((address_space(1)))
#define LAS __attribute__((address_space(3)))
typedef unsigned v4u __attribute__((ext_vector_type(4)));
typedef short v4s __attribute__((ext_vector_type(4)));
typedef short bf16x8 __attribute__((ext_vector_type(8)));
typedef float f32x4 __attribute__((ext_vector_type(4)));
typedef float f32x16 __attribute__((ext_vector_type(16)));
typedef float v2f __attribute__((ext_vector_type(2)));
typedef __bf16 v2bf __attribute__((ext_vector_type(2)));
typedef unsigned long long u64;

namespace {
constexpr unsigned FSTR = 4224;
constexpr unsigned WS_F1 = 65536, WS_F2 = WS_F1 + 16 * FSTR, WS_F3 = WS_F2 + 16 * FSTR;
constexpr unsigned WS_ST = 32768;
constexpr unsigned WS_QKV = 327680, QKV_T = 168 * 512;
constexpr unsigned WS_HO = WS_QKV + 64 * QKV_T, HO_T = 288 * 512;
constexpr unsigned WS_END = WS_HO + 64 * HO_T;
constexpr unsigned WS_DBG = 32u << 20;
constexpr u64 TAG_STEP = 0x9E3779B97F4A7C15ull;
constexpr int AUX_SC1 = 16;
constexpr unsigned SLOT = 40960, UNIT = 16384;
constexpr unsigned L_CSA = 122880, L_CSB = L_CSA + 3072, L_CNT = L_CSB + 2048;
constexpr unsigned CODE_BYTES = 20480;
constexpr unsigned OOB = 0x40000000u;
constexpr unsigned bunit(int s) { return (s == 0 ? 8 : s == 1 ? 9 : s == 2 ? 0 : s == 3 ? 1 : s == 4 ? 3 : s == 5 ? 4 : 5) * UNIT; }
constexpr unsigned wunit(int i) {
  return i == 18 ? 7 * UNIT : (i % 9) == 0 ? 2 * UNIT : (i % 9) == 1 ? 6 * UNIT : bunit(i % 9 - 2);
}
constexpr int PRO = 10;
constexpr int G_ST = 19, G_POLL = 50, G_ATT = 54, G_F1 = 42;
constexpr int avail(int g) {
  return g < 2 ? 20 : g < 6 ? 25 : g < 10 ? 30 : g < 14 ? 35 : g < 18 ? 39 : g < 22 ? 43 : g < 26 ? 49 : 53 + 2 * ((g - 26) / 4);
}
constexpr int cum(int g) {
  int c = PRO;
  for (int k = 0; k <= g; ++k) {
    int n = c + (k < 28 ? ((k & 1) && k != 27 ? 1 : 2) : 1);
    c = n < avail(k) ? n : avail(k);
  }
  return c;
}
static_assert(cum(55) == 67 && cum(27) == 53, "every item issued; B6 and W1 in front of the first store");
constexpr int rew(int g) { return g == 36 || g == 37 ? 9 : g == 40 || g == 41 ? 10 : g == 48 || g == 49 ? 11 : -1; }
constexpr int rreg(int g) { return g == 33 ? 0 : g == 37 ? 1 : g == 45 ? 2 : g == 49 ? 3 : -1; }
constexpr int nst(int g) { return g == 28 || g == 29 || g == 31 || g == 33 ? 1 : 0; }
constexpr int nextra(int g) {
  return (g == G_ST) + (rew(g) >= 0) + (g == G_ATT ? 3 : g == G_ATT + 1 ? 2 : 0);
}
constexpr int nops(int g) {
  int c = PRO + 3;
  for (int k = 0; k <= g; ++k) c += nst(k) + cum(k) - cum(k - 1) + nextra(k);
  return c;
}
constexpr int rankI(int it) {
  if (it < PRO) return it < 5 ? it + 1 : it + 2;
  int g = 0;
  while (cum(g) <= it) ++g;
  return nops(g - 1) + nst(g) + it - cum(g - 1);
}
constexpr int imin(int a, int b) { return a < b ? a : b; }
constexpr unsigned wbase(int g) {
  return g < 28 ? (((g >> 2) & 2) && g >= 0 ? 2 * SLOT + 16384 : 16384) : g < 36 ? 8 * UNIT : g < 44 ? 0 : 3 * UNIT;
}
constexpr bool isW(int it) { return it == 47 || it == 48 || it >= 51; }

template <int N> using IC = std::integral_constant<int, N>;
template <int... I, class F> __device__ __forceinline__ void sfor_(std::integer_sequence<int, I...>, F&& f) { (f(std::integral_constant<int, I>{}), ...); }
template <int N, class F> __device__ __forceinline__ void sfor(F&& f) { sfor_(std::make_integer_sequence<int, N>{}, f); }

#define WAITL0() asm volatile("s_waitcnt lgkmcnt(0)" ::: "memory")
#define BAR() do { asm volatile("" ::: "memory"); __builtin_amdgcn_s_barrier(); asm volatile("" ::: "memory"); } while (0)
#define SCHED() __builtin_amdgcn_sched_barrier(0)
#define PRIO(n) __builtin_amdgcn_s_setprio(n)
template <class T> __device__ __forceinline__ void TIE(T& v) { asm volatile("" : "+v"(v)); }
template <int N> __device__ __forceinline__ void waitv() { asm volatile("s_waitcnt vmcnt(%0)" ::"i"(N) : "memory"); }

typedef __amdgpu_buffer_rsrc_t rsrc_t;
__device__ __forceinline__ rsrc_t mkrs(const char* p, unsigned bytes) { return __builtin_amdgcn_make_buffer_rsrc((void*)p, 0, bytes, 0x00020000); }
template <unsigned L, unsigned S> __device__ __forceinline__ void dmac(unsigned voff, rsrc_t rs, unsigned ldsw) {
  unsigned t;
  asm volatile("s_add_u32 m0, %3, %4\n\ts_mov_b32 %0, %5\n\tbuffer_load_dwordx4 %1, %2, %0 offen lds"
               : "=&s"(t) : "v"(voff), "s"(rs), "s"(ldsw), "i"(L), "i"(S) : "memory", "scc");
}
template <unsigned L, unsigned S> __device__ __forceinline__ void dmam(unsigned voff, rsrc_t rs, unsigned ldsw, unsigned pitch) {
  unsigned t;
  asm volatile("s_add_u32 m0, %3, %4\n\ts_mul_i32 %0, %6, %5\n\tbuffer_load_dwordx4 %1, %2, %0 offen lds"
               : "=&s"(t) : "v"(voff), "s"(rs), "s"(ldsw), "i"(L), "i"(S), "s"(pitch) : "memory", "scc");
}
template <unsigned L, unsigned S, unsigned A> __device__ __forceinline__ void dmaa(unsigned voff, rsrc_t rs, unsigned ldsw, unsigned pitch) {
  unsigned t;
  asm volatile("s_add_u32 m0, %3, %4\n\ts_mul_i32 %0, %6, %5\n\ts_add_u32 %0, %0, %7\n\tbuffer_load_dwordx4 %1, %2, %0 offen lds"
               : "=&s"(t) : "v"(voff), "s"(rs), "s"(ldsw), "i"(L), "i"(S), "s"(pitch), "i"(A) : "memory", "scc");
}
template <unsigned S> __device__ __forceinline__ void ld16(v4u& d, unsigned voff, rsrc_t rs) {
  unsigned t;
  asm volatile("s_mov_b32 %1, %4\n\tbuffer_load_dwordx4 %0, %2, %3, %1 offen sc1" : "=v"(d), "=&s"(t) : "v"(voff), "s"(rs), "i"(S) : "memory");
}
template <unsigned S0, unsigned S1> __device__ __forceinline__ void ld16x2c(v4u& d0, v4u& d1, unsigned voff, rsrc_t rs, unsigned c) {
  unsigned t;
  asm volatile("s_cmp_eq_u32 %7, 0\n\ts_cbranch_scc1 1f\n\ts_mov_b32 %2, %5\n\tbuffer_load_dwordx4 %0, %3, %4, %2 offen sc1\n\t"
               "s_mov_b32 %2, %6\n\tbuffer_load_dwordx4 %1, %3, %4, %2 offen sc1\n1:"
               : "=&v"(d0), "=&v"(d1), "=&s"(t) : "v"(voff), "s"(rs), "i"(S0), "i"(S1), "s"(c) : "memory", "scc");
}
template <unsigned L, unsigned S, unsigned A> __device__ __forceinline__ void dmaac(unsigned voff, rsrc_t rs, unsigned ldsw, unsigned pitch, unsigned c) {
  unsigned t;
  asm volatile("s_cmp_eq_u32 %8, 0\n\ts_cbranch_scc1 1f\n\ts_add_u32 m0, %3, %4\n\ts_mul_i32 %0, %6, %5\n\ts_add_u32 %0, %0, %7\n\t"
               "buffer_load_dwordx4 %1, %2, %0 offen lds\n1:"
               : "=&s"(t) : "v"(voff), "s"(rs), "s"(ldsw), "i"(L), "i"(S), "s"(pitch), "i"(A), "s"(c) : "memory", "scc");
}
template <int N1, int N0> __device__ __forceinline__ void waitc(unsigned c) {
  static_assert(N0 <= N1 && N0 >= 0, "");
  asm volatile("s_cmp_lg_u32 %0, 0\n\ts_cbranch_scc1 1f\n\ts_waitcnt vmcnt(%2)\n1:\n\ts_waitcnt vmcnt(%1)" ::"s"(c), "i"(N1), "i"(N0) : "memory", "scc");
}
__device__ __forceinline__ void ld8(u64& d, unsigned voff, rsrc_t rs) {
  asm volatile("buffer_load_dwordx2 %0, %1, %2, 0 offen sc1" : "=v"(d) : "v"(voff), "s"(rs) : "memory");
}
__device__ __forceinline__ float wave_sum63(float v) {
#define DPP_ADD(ctrl, rm) v += __builtin_bit_cast(float, __builtin_amdgcn_update_dpp(0, __builtin_bit_cast(int, v), ctrl, rm, 0xf, true));
  DPP_ADD(0x111, 0xf) DPP_ADD(0x112, 0xf) DPP_ADD(0x114, 0xf) DPP_ADD(0x118, 0xf) DPP_ADD(0x142, 0xa) DPP_ADD(0x143, 0xc)
#undef DPP_ADD
  return v;
}
__device__ __forceinline__ bf16x8 ldtr(unsigned a) {
  v4s lo = __builtin_amdgcn_ds_read_tr16_b64_v4i16((LAS v4s*)(uintptr_t)a);
  v4s hi = __builtin_amdgcn_ds_read_tr16_b64_v4i16((LAS v4s*)(uintptr_t)(a + 512));
  return __builtin_shufflevector(lo, hi, 0, 1, 2, 3, 4, 5, 6, 7);
}
__device__ __forceinline__ unsigned pk(float a, float b) {
  v2f f = {a, b};
  return __builtin_bit_cast(unsigned, __builtin_convertvector(f, v2bf));
}
__device__ __forceinline__ float asf(unsigned d) { return __builtin_bit_cast(float, d); }
__device__ __forceinline__ float blo(unsigned d) { return __builtin_bit_cast(float, d << 16); }
__device__ __forceinline__ float bhi(unsigned d) { return __builtin_bit_cast(float, d & 0xffff0000u); }
__device__ __forceinline__ f32x16 mm32(bf16x8 a, bf16x8 b, f32x16 c) { return __builtin_amdgcn_mfma_f32_32x32x16_bf16(a, b, c, 0, 0, 0); }
__device__ __forceinline__ f32x4 mm16(bf16x8 a, bf16x8 b, f32x4 c) { return __builtin_amdgcn_mfma_f32_16x16x32_bf16(a, b, c, 0, 0, 0); }
__device__ __forceinline__ bf16x8 asb(v4u v) { return __builtin_bit_cast(bf16x8, v); }

__device__ __forceinline__ unsigned asu(float f) { return __builtin_bit_cast(unsigned, f); }
template <bool MAX> __device__ __forceinline__ float rows4(float v) {
  {
    const auto r = __builtin_amdgcn_permlane16_swap(asu(v), asu(v), false, false);
    const unsigned a = r[0], b = r[1];
    v = MAX ? fmaxf(asf(a), asf(b)) : asf(a) + asf(b);
  }
  const auto r = __builtin_amdgcn_permlane32_swap(asu(v), asu(v), false, false);
  const unsigned a = r[0], b = r[1];
  return MAX ? fmaxf(asf(a), asf(b)) : asf(a) + asf(b);
}
__device__ __forceinline__ void arrive(unsigned cnt, unsigned foff, rsrc_t rs, u64 tag) {
  unsigned a = cnt, t;
  u64 tv = tag;
  asm volatile("s_mov_b64 exec, 1\n\tv_mov_b32 %0, 1\n\tds_add_rtn_u32 %0, %1, %0\n\ts_waitcnt lgkmcnt(0)\n\t"
               "v_cmp_eq_u32 vcc, 7, %0\n\ts_cbranch_vccz 1f\n\tbuffer_store_dwordx2 %2, off, %3, %4 sc1\n1:\n\ts_mov_b64 exec, -1"
               : "=&v"(t) : "v"(a), "v"(tv), "s"(rs), "s"(foff) : "memory", "vcc");
}
__device__ __forceinline__ void ld8c(u64& d, unsigned voff, rsrc_t rs, unsigned c) {
  asm volatile("s_cmp_eq_u32 %3, 0\n\ts_cbranch_scc1 1f\n\tbuffer_load_dwordx2 %0, %1, %2, 0 offen sc1\n1:" : "=&v"(d) : "v"(voff), "s"(rs), "s"(c) : "memory", "scc");
}
__device__ __forceinline__ void poll(u64 v, unsigned voff, rsrc_t rs, u64 tag, unsigned c) {
  unsigned n = 1u << 19;
  asm volatile("s_cmp_eq_u32 %5, 0\n\ts_cbranch_scc1 2f\n\ts_waitcnt vmcnt(0)\n1:\n\tv_cmp_ne_u64 vcc, %4, %0\n\ts_cbranch_vccz 2f\n\ts_sleep 3\n\t"
               "buffer_load_dwordx2 %0, %2, %3, 0 offen sc1\n\ts_sub_u32 %1, %1, 1\n\ts_waitcnt vmcnt(0)\n\ts_cbranch_scc0 1b\n2:"
               : "+v"(v), "+s"(n) : "v"(voff), "s"(rs), "s"(tag), "s"(c) : "memory", "vcc", "scc");
}
__device__ __forceinline__ void waitw(unsigned lds) {
  unsigned a = lds, t, n = 1u << 22;
  asm volatile("1:\n\tds_read_b32 %0, %2\n\ts_waitcnt lgkmcnt(0)\n\tv_cmp_eq_u32 vcc, 0, %0\n\ts_cbranch_vccz 2f\n\ts_sleep 1\n\t"
               "s_sub_u32 %1, %1, 1\n\ts_cbranch_scc0 1b\n2:"
               : "=&v"(t), "+s"(n) : "v"(a) : "memory", "vcc", "scc");
}
__device__ __forceinline__ void pollg(v4u& v, unsigned voff, rsrc_t rs, u64 tag) {
  unsigned n = 1u << 20, v1 = v[1], v3 = v[3], t;
  const unsigned lo = (unsigned)tag, hi = (unsigned)(tag >> 32);
  if (__all(v1 == lo && v3 == hi)) return;
#pragma unroll 1
  for (; n; --n) {
    v = __builtin_amdgcn_raw_buffer_load_b128(rs, voff, 0, AUX_SC1);
    v1 = v[1]; v3 = v[3];
    if (__all(v1 == lo && v3 == hi)) break;
    __builtin_amdgcn_s_sleep(1);
  }
  (void)t;
}
__device__ __forceinline__ bf16x8 csfrag(unsigned lds) {
  const f32x4 p = *(const LAS f32x4*)(uintptr_t)lds;
  const float c = (p[0] + p[1]) + (p[2] + p[3]);
  const unsigned h = pk(c, 0.f);
  const unsigned d = pk(c, c - blo(h));
  return asb(v4u{d, d, 0u, 0u});
}
template <int C> __device__ __forceinline__ void store_q(const f32x16& a, float rstd, rsrc_t rs, unsigned off) {
  v4u o;
#pragma unroll
  for (int e = 0; e < 4; ++e) o[e] = pk(a[8 * C + 2 * e] * rstd, a[8 * C + 2 * e + 1] * rstd);
  __builtin_amdgcn_raw_buffer_store_b128(o, rs, off + C * 1024, 0, AUX_SC1);
}
template <int Q> __device__ __forceinline__ void swiglu4(const f32x16& g, const f32x16& u, float rstd, v4u& o) {
  const float k1 = -1.4426950408889634f * rstd, k2 = rstd * rstd;
  float h[4];
#pragma unroll
  for (int e = 0; e < 4; ++e) {
    const float gg = g[4 * Q + e];
    const float r = __builtin_amdgcn_rcpf(1.0f + __builtin_amdgcn_exp2f(gg * k1));
    h[e] = (gg * k2) * (u[4 * Q + e] * r);
  }
  o[2 * (Q & 1)] = pk(h[0], h[1]);
  o[2 * (Q & 1) + 1] = pk(h[2], h[3]);
}
#define ENC 1
#define STAMP(i) do { if (i == 0 || i == ENC) { u64 t_; SCHED(); asm volatile("s_memrealtime %0\n\ts_waitcnt lgkmcnt(0)" : "=s"(t_)::"memory"); SCHED(); est[i == 0 ? 0 : 1] = t_; } } while (0)
#define CSTAMP(i)
#define STAMPB(i)
#define STAMPN(i) STAMP(i)
}

__global__ __launch_bounds__(512, 2) void fused_block(const char* __restrict__ x, const char* __restrict__ wqkv, const char* __restrict__ wo,
                                                      const char* __restrict__ wg, const char* __restrict__ wu, const char* __restrict__ wd,
                                                      char* __restrict__ out, char* __restrict__ ws) {
  u64 est[2];
  STAMP(0);
  asm volatile("" ::"s"(x), "s"(wqkv), "s"(wo), "s"(wg), "s"(wu), "s"(wd), "s"(out), "s"(ws));
  const int tid = threadIdx.x, lane = tid & 63, w = __builtin_amdgcn_readfirstlane(tid >> 6);
  const int rg = w & 3, set = w >> 2, rr = rg ^ (2 * set);
  const int b = blockIdx.x, xl = b & 7, idx = b >> 3;
  const int j = (idx & 3) < 3 ? 3 * (xl & 3) + (idx & 3) : 12 + (xl & 3), p = 8 * (xl >> 2) + (idx >> 2);
  const bool typeA = j < 12;
  const bool p2 = idx < 28;
  const int tok = lane & 31, hh = lane >> 5;
  const unsigned lw1 = w * 1024, lw2 = w * 2048;
  const rsrc_t rs = mkrs(ws, WS_END), rx = mkrs(x, 2048 * 896);

  const int ts = 128 * p + 8 * j + w;
  v4u xrow;
  unsigned warm;
  asm volatile("buffer_load_dwordx4 %0, %1, %2, 0 offen" : "=v"(xrow) : "v"(lane < 56 ? ts * 896 + lane * 16 : OOB), "s"(rx) : "memory");
  const unsigned l3 = lane >> 3, l7 = lane & 7;
  const unsigned cw = (l7 ^ (((lane >> 4) & 1) << 2)) * 16;
  const unsigned vx0 = (128 * p + 16 * w + l3) * 896 + ((l7 ^ (lane >> 4)) * 16), vx1 = (vx0 ^ 64) + 8 * 896;
  const int qb0 = typeA ? j : (j == 15 ? 18 : 3 * j - 24);
  const unsigned vq = (8 * w + l3) * 2688 + qb0 * 128 + cw, vf = (8 * w + l3) * 3584 + j * 128 + cw, v12 = typeA ? vf : vq;
  const rsrc_t rq = mkrs(wqkv, 448 * 2688), rgt = mkrs(wg, 448 * 3584), rup = mkrs(wu, 448 * 3584);
  const rsrc_t r1 = mkrs(typeA ? wg : wqkv + 128, typeA ? 448 * 3584 : 448 * 2688 - 128);
  const rsrc_t r2 = mkrs(typeA ? wu : wqkv + 256, typeA ? 448 * 3584 : 448 * 2688 - 256);
  const unsigned st12 = typeA ? 64 * 3584 : 64 * 2688;
  auto itemA = [&](auto it_) {
    constexpr int it = it_, s = it / 5, k = it % 5;
    constexpr unsigned sl = (s & 3) * SLOT;
    if constexpr (k == 0) dmac<sl, s * 128>(vx0, rx, lw2);
    else if constexpr (k == 1) dmac<sl + 1024, s * 128>(vx1, rx, lw2);
    else if constexpr (k == 2) dmac<sl + 16384, s * 64 * 2688>(vq, rq, lw1);
    else if constexpr (k == 3) dmam<sl + 24576, s>(v12, r1, lw1, st12);
    else dmam<sl + 32768, s>(v12, r2, lw1, st12);
  };
  sfor<5>([&](auto it) { itemA(it); });
  {
    u64 pc;
    asm volatile("s_getpc_b64 %0" : "=s"(pc));
    const unsigned o = ((idx * 8 + w) * 8 + (lane & 7)) * 128 % CODE_BYTES;
    asm volatile("global_load_dword %0, %1, %2" : "=v"(warm) : "v"(o), "s"(pc) : "memory");
  }
  sfor<PRO - 5>([&](auto it) { itemA(IC<5 + it>{}); });
  SCHED();
  const int rg2 = p2 ? 4 * xl + idx / 7 : 2 * p, cg = idx % 7;
  const bool owave = w >= 6;
  const unsigned cO = (w + 2) & 8, cW = cO ^ 8, cH = __builtin_amdgcn_readfirstlane(p2 ? cW : 0u), c18 = (w & 4) ^ 4, cC = (j + 4) & 16, c7 = (w + 1) & 8;
  const int ksA = owave ? 122 + 16 * (w - 6) : 8 * w;
  const int ksB = owave ? 112 + 16 * (w - 6) : 48 + (w < 5 ? 11 * w : 54);
  const unsigned pstr = owave ? 28 * 896 : 32 * 896;
  const rsrc_t rw2 = mkrs(owave ? wo : wd, owave ? 448 * 896 : 1792 * 896);
  const unsigned vdA = p2 ? (owave ? (w - 6) * 224 + 140 : ksA * 16) * 896 + l3 * 896 + cg * 128 + cw : OOB;
  const unsigned vdB = p2 ? (owave ? (w - 6) * 224 : ksB * 16) * 896 + l3 * 896 + cg * 128 + cw : OOB;
  auto issueWh = [&](auto i_, auto hf_) {
    constexpr int i = i_, hf = hf_;
    constexpr unsigned L = wunit(i) + hf * 1024, S = (i & 7) >> 1 | (i >= 16 ? 4 : 0), A = ((i & 1) * 16 + hf * 8) * 896;
    if constexpr (i == 6 || i == 7) dmaac<L, S, A>(vdA, rw2, lw2, pstr, cW);
    else if constexpr (i == 18) dmaac<L, S, A>(vdB, rw2, lw2, pstr, c18);
    else dmaa<L, S, A>(i < 8 ? vdA : vdB, rw2, lw2, pstr);
  };
  auto issueW = [&](auto i) { issueWh(i, IC<0>{}); issueWh(i, IC<1>{}); };
  auto item = [&](auto it_) {
    constexpr int it = it_;
    if constexpr (it < 35) {
      itemA(it_);
    } else if constexpr (isW(it)) {
      issueWh(IC<(it < 49 ? 0 : (it - 49) >> 1)>{}, IC<((it - 47) & 1)>{});
    } else {
      constexpr int s = it < 47 ? (it - 35) >> 1 : 6, k = (it - 35) & 1;
      dmac<bunit(s) + k * 8192, s * 64 * 3584 + 12 * 128>(vf, k ? rup : rgt, lw1);
    }
  };
  u64 tag;
  asm volatile("s_load_dwordx2 %0, %1, 0x0\n\ts_waitcnt lgkmcnt(0)" : "=s"(tag) : "s"(ws) : "memory");
  tag += TAG_STEP;
  waitv<PRO + 1>();
  TIE(xrow);
  {
    float a = 0.f, q = 0.f;
#pragma unroll
    for (int e = 0; e < 4; ++e) {
      const float lo = blo(xrow[e]), hi = bhi(xrow[e]);
      a += lo + hi; q = fmaf(lo, lo, fmaf(hi, hi, q));
    }
    a = wave_sum63(a); q = wave_sum63(q);
    const float m = a * (1.0f / 448.0f);
    const float r = __builtin_amdgcn_rsqf(fmaxf(q * (1.0f / 448.0f) - m * m, 0.f) + 1e-5f);
    const v4u gr = {__builtin_bit_cast(unsigned, m), (unsigned)tag, __builtin_bit_cast(unsigned, r), (unsigned)(tag >> 32)};
    __builtin_amdgcn_raw_buffer_store_b128(gr, rs, lane == 63 ? WS_ST + ts * 16 : OOB, 0, AUX_SC1);
  }

  const unsigned q4 = (lane & 15) >> 2, p4 = lane & 3, sp4 = ((p4 & 1) << 1) | (p4 >> 1);
  const unsigned trl = (8 * hh + q4) * 128 + (4 * ((lane >> 4) & 1) + sp4) * 8;
  const unsigned aw = (trl + ((set ^ (q4 >> 1)) << 6)) | (rr << 11);
  const unsigned ax = (32 * rg + tok) * 128 + (((2 * rr + hh) ^ ((tok >> 1) & 7)) << 4);
  const unsigned selw = (((lane >> 4) ^ (lane >> 2)) & 1) ? 0u : 0x3F803F80u;
  const bf16x8 sel = asb(v4u{selw, selw, selw, selw});
  unsigned bw[4], bx[4];
#pragma unroll
  for (int i = 0; i < 4; ++i) { bw[i] = (aw ^ (i * 2048)) + 16384; bx[i] = ax ^ (i * 32); TIE(bw[i]); TIE(bx[i]); }

  bf16x8 xf[28];
  f32x16 acc[3], accB[2];
  f32x4 cs[3], csB[2];
#pragma unroll
  for (int t = 0; t < 3; ++t) { acc[t] = 0.f; cs[t] = 0.f; }
#pragma unroll
  for (int t = 0; t < 2; ++t) { accB[t] = 0.f; csB[t] = 0.f; }
  bf16x8 wf[3][3];
  auto rdX = [&](auto n_) {
    constexpr int n = n_, s = n >> 2, i = n & 3;
    if constexpr (s == 2 || s == 6) { bx[i] += 2 * SLOT; TIE(bx[i]); }
    if constexpr (s == 4) { bx[i] -= 2 * SLOT; TIE(bx[i]); }
    xf[n] = *(const LAS bf16x8*)(uintptr_t)(bx[i] + (s & 1) * SLOT);
  };
  auto rdW = [&](auto g_, auto t_) {
    constexpr int g = g_, t = t_, i = g & 3;
    if constexpr (t == 0 && wbase(g) != wbase(g - 4)) { bw[i] += wbase(g) - wbase(g - 4); TIE(bw[i]); }
    constexpr unsigned off = (g < 28 ? ((g >> 2) & 3) * SLOT + 16384 : bunit((g - 28) >> 2)) + t * 8192 - wbase(g);
    static_assert(off + 512 < 65536, "ds offset");
    wf[g % 3][t] = ldtr(bw[i] + off);
  };
  const unsigned tt1 = 4 * p + rg;
  const unsigned vl = lane * 16;
  v4u stg;
  float rstd;
  bf16x8 mu, cf[3];

  waitv<PRO - 5 + 1>();
  TIE(warm);
  BAR();
  STAMP(1); CSTAMP(12);
  sfor<2>([&](auto n) { rdX(n); rdW(n, IC<0>{}); rdW(n, IC<1>{}); rdW(n, IC<2>{}); });
  sfor<28>([&](auto n_) {
    constexpr int n = n_, s = n >> 2, i = n & 3, c0 = cum(n - 1), c1 = cum(n);
    constexpr int NT = n < 26 ? 3 : 2;
    SCHED();
    if constexpr (i != 2) PRIO(3 - ((i + 2) & 3));
    acc[0] = mm32(wf[n % 3][0], xf[n], acc[0]);
    SCHED();
    if constexpr (i == 2) {
      if constexpr (s == 4) { if (tid < 4) *(LAS unsigned*)(uintptr_t)(L_CNT + tid * 4) = 0u; }
      if constexpr (s == 6) {
#pragma unroll
        for (int t = 0; t < 3; ++t) *(LAS float*)(uintptr_t)(L_CSA + ((set * 3 + t) * 32 + tok) * 16 + rg * 4) = cs[t][0];
      }
      WAITL0();
      static_assert(c0 > (s < 6 ? 5 * s + 9 : 36), "next stage issued");
      constexpr int y = nops(n - 1) - 1 - rankI(s < 6 ? 5 * s + 9 : 36);
      waitv<s == 6 ? imin(y, nops(n - 1) - nops(G_ST)) : y>();
      BAR();
      PRIO(3);
    }
    if constexpr (n < 26) rdX(IC<n + 2>{});
    rdW(IC<n + 2>{}, IC<0>{});
    if constexpr (i == 1) { rdW(IC<n + 2>{}, IC<1>{}); rdW(IC<n + 2>{}, IC<2>{}); }
    if constexpr (n == 26) {
      TIE(stg);
      pollg(stg, WS_ST + (tt1 * 32 + tok) * 16, rs, tag);
      rstd = asf(stg[2]);
      const float m = asf(stg[0]);
      const unsigned mh = pk(-m, -m);
      const float ml = -m - blo(mh);
      mu = asb(v4u{hh ? 0u : mh, hh ? 0u : pk(ml, ml), 0u, 0u});
      TIE(mu);
    }
    if constexpr (n == 27) { cf[2] = csfrag(L_CSA + ((set * 3 + 2) * 32 + tok) * 16); TIE(cf[2]); }
    SCHED();
    if constexpr (i == 0) cs[0] = mm16(sel, wf[n % 3][0], cs[0]);
    if constexpr (n == 27) acc[0] = mm32(cf[0], mu, acc[0]);
    acc[1] = mm32(wf[n % 3][1], xf[n], acc[1]);
    SCHED();
    if constexpr (i != 1) rdW(IC<n + 2>{}, IC<1>{});
    if constexpr (c1 > c0) item(IC<c0>{});
    if constexpr (n == 26) { cf[0] = csfrag(L_CSA + ((set * 3 + 0) * 32 + tok) * 16); TIE(cf[0]); }
    SCHED();
    if constexpr (i == 0) cs[1] = mm16(sel, wf[n % 3][1], cs[1]);
    if constexpr (n == 27) acc[1] = mm32(cf[1], mu, acc[1]);
    acc[2] = mm32(wf[n % 3][2], xf[n], acc[2]);
    SCHED();
    if constexpr (i != 1 && NT == 3) rdW(IC<n + 2>{}, IC<2>{});
    if constexpr (c1 > c0 + 1) item(IC<c0 + 1>{});
    if constexpr (n == G_ST) ld16<WS_ST>(stg, (tt1 * 32 + tok) * 16, rs);
    if constexpr (n == 26) { cf[1] = csfrag(L_CSA + ((set * 3 + 1) * 32 + tok) * 16); TIE(cf[1]); }
    SCHED();
    if constexpr (i == 0) cs[2] = mm16(sel, wf[n % 3][2], cs[2]);
    if constexpr (n == 27) acc[2] = mm32(cf[2], mu, acc[2]);
  });
  SCHED();
  STAMP(2); CSTAMP(13);
  const unsigned oQ = j < 15 ? vl + WS_QKV + tt1 * QKV_T + (2 * qb0 + set) * 2048 : OOB;
  const unsigned oH = vl + WS_HO + tt1 * HO_T + (2 * j + set) * 2048;
  const int n16 = lane & 15, g4 = lane >> 4, odd = j & 1;
  const int tq = 128 * p + 16 * w + n16, tk = tq >= 16 ? tq - 16 : tq;
  const unsigned oq = (tq >> 5) * QKV_T + (tq & 31) * 16 + (g4 + ((7 * j) >> 1)) * 512;
  const unsigned ok = (tk >> 5) * QKV_T + (tk & 31) * 16 + (g4 + ((7 * j) >> 1)) * 512;
  const unsigned fo1 = WS_F1 + (g4 == 1 && p > 0 ? p - 1 : g4 == 2 ? rg2 >> 1 : p) * FSTR + n16 * 8;
  const unsigned a2 = w * 2048 + trl + ((q4 >> 1) << 6);
  u64 fl;
  v4u qf, k0, k1, v0, v1, hv;
  bf16x8 w2[9][2];
  auto toReg = [&](auto i) {
    w2[i][0] = ldtr(a2 + wunit(i)); w2[i][1] = ldtr((a2 ^ 64) + wunit(i));
  };
  sfor<28>([&](auto n_) {
    constexpr int n = n_, s = n >> 2, i = n & 3, g = 28 + n, c0 = cum(g - 1), c1 = cum(g);
    SCHED();
    if constexpr (i != 2) PRIO(3 - ((i + 2) & 3));
    accB[0] = mm32(wf[g % 3][0], xf[n], accB[0]);
    SCHED();
    if constexpr (i == 2) {
      if constexpr (s == 6) {
#pragma unroll
        for (int t = 0; t < 2; ++t) *(LAS float*)(uintptr_t)(L_CSB + ((set * 2 + t) * 32 + tok) * 16 + rg * 4) = csB[t][0];
      }
      WAITL0();
      if constexpr (g == G_F1) STAMPB(6);
      if constexpr (s == 0) waitv<nops(g - 1) - 1 - rankI(48)>();
      if constexpr (s == 1) {
        waitc<nops(g - 1) - 1 - rankI(52) + 2, nops(g - 1) - 1 - rankI(52)>(cC);
      }
      if constexpr (g == G_F1) waitv<nops(g - 1) - 1 - nops(32)>();
      if constexpr (g == 46) waitv<nops(g - 1) - 1 - rankI(56)>();
      if constexpr (g == G_ATT) { STAMPB(8); poll(fl, fo1, rs, tag, c7); }
      BAR();
      if constexpr (g == G_ATT) STAMPB(9);
      PRIO(3);
      if constexpr (s == 1) STAMPB(5);
      if constexpr (g == G_F1) {
        STAMPB(7);
        if (tid == 0) __hip_atomic_store((GAS u64*)(ws + (WS_F1 + p * FSTR + j * 8)), tag, __ATOMIC_RELAXED, __HIP_MEMORY_SCOPE_AGENT);
      }
    }
    if constexpr (n < 26) {
      rdW(IC<g + 2>{}, IC<0>{});
      if constexpr (i == 1) rdW(IC<g + 2>{}, IC<1>{});
    }
    if constexpr (rreg(g) >= 0) toReg(IC<rreg(g)>{});
    if constexpr (n == 0) store_q<0>(acc[0], rstd, rs, oQ);
    if constexpr (n == 1) store_q<1>(acc[0], rstd, rs, oQ);
    if constexpr (n >= 2 && n <= 5) {
      constexpr int Q = n - 2;
      if (typeA) {
        swiglu4<Q>(acc[1], acc[2], rstd, hv);
        if constexpr (Q & 1) __builtin_amdgcn_raw_buffer_store_b128(hv, rs, oH + (Q >> 1) * 1024, 0, AUX_SC1);
        else TIE(hv);
      } else store_q<(Q & 1)>(acc[1 + (Q >> 1)], rstd, rs, oQ + (1 + (Q >> 1)) * 4096);
    }
    if constexpr (n == 26) { cf[0] = csfrag(L_CSB + ((set * 2 + 0) * 32 + tok) * 16); TIE(cf[0]); }
    if constexpr (n == 27) { cf[1] = csfrag(L_CSB + ((set * 2 + 1) * 32 + tok) * 16); TIE(cf[1]); }
    SCHED();
    if constexpr (i == 0) csB[0] = mm16(sel, wf[g % 3][0], csB[0]);
    if constexpr (n == 27) accB[0] = mm32(cf[0], mu, accB[0]);
    accB[1] = mm32(wf[g % 3][1], xf[n], accB[1]);
    SCHED();
    if constexpr (n < 26 && i != 1) rdW(IC<g + 2>{}, IC<1>{});
    sfor<c1 - c0>([&](auto k) { item(IC<c0 + k>{}); });
    if constexpr (rew(g) >= 0) issueWh(IC<rew(g)>{}, IC<(g & 1)>{});
    if constexpr (g == G_ATT) {
      ld16<WS_QKV>(qf, oq, rs);
      ld16<WS_QKV + 56 * 512>(k0, ok, rs);
      ld16<WS_QKV + 56 * 512>(k1, oq, rs);
    }
    if constexpr (g == G_ATT + 1) { ld16<WS_QKV + 112 * 512>(v0, ok, rs); ld16<WS_QKV + 112 * 512>(v1, oq, rs); }
    if constexpr (g == G_POLL) ld8c(fl, fo1, rs, c7);
    SCHED();
    if constexpr (i == 0) csB[1] = mm16(sel, wf[g % 3][1], csB[1]);
    if constexpr (n == 27) accB[1] = mm32(cf[1], mu, accB[1]);
  });
  SCHED();
  PRIO(0);
  STAMP(4); CSTAMP(14);
  const unsigned vA = vl + 2 * rg2 * HO_T + ksA * 1024, vB = vl + 2 * rg2 * HO_T + ksB * 1024;
  v4u af[19][2];
  auto ldE = [&](auto i_, unsigned c) {
    constexpr int i = i_;
    ld16x2c<WS_HO + i * 1024, WS_HO + HO_T + i * 1024>(af[i][0], af[i][1], vA, rs, c);
  };
  auto ldL = [&](auto i_) {
    constexpr int i = i_;
    if constexpr (i == 18) ld16x2c<WS_HO + (i - 8) * 1024, WS_HO + HO_T + (i - 8) * 1024>(af[i][0], af[i][1], vB, rs, c18);
    else {
      ld16<WS_HO + (i - 8) * 1024>(af[i][0], vB, rs);
      ld16<WS_HO + HO_T + (i - 8) * 1024>(af[i][1], vB, rs);
    }
  };
  swiglu4<0>(accB[0], accB[1], rstd, hv); swiglu4<1>(accB[0], accB[1], rstd, hv);
  __builtin_amdgcn_raw_buffer_store_b128(hv, rs, oH + 24 * 2048, 0, AUX_SC1);
  swiglu4<2>(accB[0], accB[1], rstd, hv); swiglu4<3>(accB[0], accB[1], rstd, hv);
  __builtin_amdgcn_raw_buffer_store_b128(hv, rs, oH + 24 * 2048 + 1024, 0, AUX_SC1);
  SCHED();
  STAMPN(5);
  sfor<3>([&](auto i) { ldE(i, cH); });
  waitc<2 + 6, 2>(cH);
  TIE(qf); TIE(k0); TIE(k1); TIE(v0); TIE(v1);
  {
    if (odd && g4 == 0) { qf[0] = 0; qf[1] = 0; }
    if (!odd && g4 == 3) { qf[2] = 0; qf[3] = 0; }
    const unsigned scr = wunit(3) + w * 2048;
    *(LAS v4u*)(uintptr_t)(scr + n16 * 64 + g4 * 16) = v0;
    *(LAS v4u*)(uintptr_t)(scr + 1024 + n16 * 64 + g4 * 16) = v1;
    const f32x4 z4 = 0.f;
    const f32x4 sa = mm16(asb(k0), asb(qf), z4), sb = mm16(asb(k1), asb(qf), z4);
    bf16x8 vt[2];
#pragma unroll
    for (int t = 0; t < 2; ++t) {
      const unsigned a = scr + (4 * g4 + q4) * 64 + (((2 * p4 + t + odd) * 8) & 63);
      const v4s lo = __builtin_amdgcn_ds_read_tr16_b64_v4i16((LAS v4s*)(uintptr_t)a);
      const v4s hi = __builtin_amdgcn_ds_read_tr16_b64_v4i16((LAS v4s*)(uintptr_t)(a + 1024));
      vt[t] = __builtin_shufflevector(lo, hi, 0, 1, 2, 3, 4, 5, 6, 7);
    }
    sfor<5>([&](auto i) { toReg(IC<4 + i>{}); });
    const bool par = n16 & 1, c0 = n16 - 4 * g4 >= 0, c1 = n16 - 4 * g4 >= 2, back = tq >= 16;
    const float ninf = -3.0e38f, sc = 0.1889822365046136f * 1.4426950408889634f;
    const float s0 = c0 ? (par ? sb[1] : sb[0]) * sc : back ? (par ? sa[1] : sa[0]) * sc : ninf;
    const float s1 = c1 ? (par ? sb[3] : sb[2]) * sc : back ? (par ? sa[3] : sa[2]) * sc : ninf;
    const float mx = rows4<true>(fmaxf(s0, s1));
    const float e0 = __builtin_amdgcn_exp2f(s0 - mx), e1 = __builtin_amdgcn_exp2f(s1 - mx);
    const float inv = __builtin_amdgcn_rcpf(rows4<false>(e0 + e1));
    const unsigned u0 = par ? pk(0.f, e0 * inv) : pk(e0 * inv, 0.f), u1 = par ? pk(0.f, e1 * inv) : pk(e1 * inv, 0.f);
    const v4u pf = {c0 ? 0u : u0, c1 ? 0u : u1, c0 ? u0 : 0u, c1 ? u1 : 0u};
    const f32x4 oa = mm16(vt[0], asb(pf), z4), ob = mm16(vt[1], asb(pf), z4);
    v4u ov = {pk(oa[0], oa[1]), pk(oa[2], oa[3]), pk(ob[0], ob[1]), pk(ob[2], ob[3])};
    if (g4 == 3) { ov[2] = 0; ov[3] = 0; }
    __builtin_amdgcn_raw_buffer_store_b128(ov, rs, (tq >> 5) * HO_T + (tq & 31) * 16 + g4 * 512 + WS_HO + (224 + 4 * j) * 512, 0, AUX_SC1);
  }
  SCHED();
  STAMPN(6);
  sfor<3>([&](auto i) { ldE(IC<3 + i>{}, cH); });
  WAITL0();
  issueW(IC<12>{});
  waitc<6 + 1 + 6 + 2, 1 + 2>(cH);
  arrive(L_CNT, WS_F2 + p * FSTR + j * 8, rs, tag);
  STAMPN(7);
  sfor<2>([&](auto i) { ldE(IC<6 + i>{}, cH); });
  issueW(IC<13>{});
  waitc<6 + 2 + 4 + 2, 2 + 2>(cH);
  arrive(L_CNT + 4, WS_F3 + p * FSTR + j * 8, rs, tag);
  if (!p2) {
    if (b == 255 && w == 0) {
#pragma unroll 1
      for (int k = 0; k < 4; ++k) poll(~tag, WS_F1 + (k * 4 + g4) * FSTR + n16 * 8, rs, tag, 1u);
      if (lane == 0) __hip_atomic_store((GAS u64*)ws, tag, __ATOMIC_RELAXED, __HIP_MEMORY_SCOPE_AGENT);
    }
    return;
  }

  {
    const unsigned fo2 = (w == 6 ? WS_F3 : WS_F2) + (rg2 >> 1) * FSTR + n16 * 8;
    const unsigned vo = (64 * rg2 + 8 * w + l3) * 896 + cg * 128 + l7 * 16;
    v4u xres;
    f32x16 c2[2][2];
#pragma unroll
    for (int t = 0; t < 4; ++t) c2[t >> 1][t & 1] = 0.f;
    auto step = [&](auto i_) {
      constexpr int i = i_;
      bf16x8 wa, wb;
      if constexpr (i < 9) { wa = w2[i][0]; wb = w2[i][1]; }
      else { wa = ldtr(a2 + wunit(i)); wb = ldtr((a2 ^ 64) + wunit(i)); }
      TIE(af[i][0]); TIE(af[i][1]);
      c2[0][0] = mm32(wa, asb(af[i][0]), c2[0][0]);
      c2[0][1] = mm32(wa, asb(af[i][1]), c2[0][1]);
      c2[1][0] = mm32(wb, asb(af[i][0]), c2[1][0]);
      c2[1][1] = mm32(wb, asb(af[i][1]), c2[1][1]);
      SCHED();
    };
    ld16<0>(xres, vo, rx);
    issueW(IC<14>{});
    if (!owave) { step(IC<0>{}); step(IC<1>{}); }
    issueW(IC<15>{});
    if (!owave) { step(IC<2>{}); waitv<2 + 4 + 2 + 1 + 4>(); step(IC<3>{}); }
    issueW(IC<16>{});
    if (!owave) { step(IC<4>{}); step(IC<5>{}); }
    issueW(IC<17>{});
    issueW(IC<18>{});
    if (!owave) { waitv<2 + 1 + 4 + 4>(); step(IC<6>{}); step(IC<7>{}); }
    poll(~tag, fo2, rs, tag, cO);
    if (owave) *(LAS unsigned*)(uintptr_t)(L_CNT + 8 + (w & 1) * 4) = 1u;
    waitw(L_CNT + (owave ? 8 : 12));
    STAMPN(8);
    sfor<6>([&](auto i) { ldE(i, cO); });
    sfor<4>([&](auto i) { ldL(IC<8 + i>{}); });
    sfor<6>([&](auto i) {
      if (owave) { waitv<18>(); step(i); }
      ldL(IC<12 + i>{});
    });
    ldL(IC<18>{});
    sfor<10>([&](auto i) { waitv<2 * (9 - i)>(); step(IC<8 + i>{}); });
    if (w < 4) step(IC<18>{});
    STAMPN(9);
#pragma unroll
    for (int nt = 0; nt < 2; ++nt)
#pragma unroll
      for (int tt = 0; tt < 2; ++tt)
#pragma unroll
        for (int c = 0; c < 2; ++c) {
          v4u o;
#pragma unroll
          for (int e = 0; e < 4; ++e) o[e] = pk(c2[nt][tt][8 * c + 2 * e], c2[nt][tt][8 * c + 2 * e + 1]);
          *(LAS v4u*)(uintptr_t)((2 * nt + c) * UNIT + w * 2048 + hh * 1024 + (((32 * tt + tok) ^ (4 * (2 * c + hh))) * 16)) = o;
        }
    WAITL0();
    BAR();
    STAMP(10);
    float f[8], sv[8];
#pragma unroll
    for (int e = 0; e < 8; ++e) { f[e] = 0.f; sv[e] = 0.f; }
    const unsigned ar = (l7 >> 1) * UNIT + (l7 & 1) * 1024 + (((8 * w + l3) ^ (4 * (l7 & 3))) * 16);
#pragma unroll
    for (int k = 0; k < 8; ++k) {
      const v4u v = *(const LAS v4u*)(uintptr_t)(ar + k * 2048);
#pragma unroll
      for (int e = 0; e < 4; ++e) {
        if (k < 6) { f[2 * e] += blo(v[e]); f[2 * e + 1] += bhi(v[e]); }
        else { sv[2 * e] += blo(v[e]); sv[2 * e + 1] += bhi(v[e]); }
      }
    }
    TIE(xres);
    float r[8];
#pragma unroll
    for (int e = 0; e < 8; ++e) {
      const float sg = f[e] * __builtin_amdgcn_rcpf(1.0f + __builtin_amdgcn_exp2f(f[e] * -1.4426950408889634f));
      r[e] = fmaf(sv[e], sg, (e & 1) ? bhi(xres[e >> 1]) : blo(xres[e >> 1]));
    }
    const v4u o = {pk(r[0], r[1]), pk(r[2], r[3]), pk(r[4], r[5]), pk(r[6], r[7])};
    *(GAS v4u*)(out + vo) = o;
    STAMP(11); CSTAMP(15);
    while (__builtin_amdgcn_s_memrealtime() - est[0] < 6000 + 10 * (est[1] - est[0])) __builtin_amdgcn_s_sleep(1);
  }
}

extern "C" void kernel_launch(void* const* d_in, const int* in_sizes, int n_in, void* d_out, int out_size, void* d_ws, size_t ws_size,
                              hipStream_t stream) {
  (void)in_sizes; (void)n_in; (void)out_size; (void)ws_size;
  (void)hipFuncSetAttribute((const void*)fused_block, hipFuncAttributeMaxDynamicSharedMemorySize, 163840);
  fused_block<<<256, 512, 163840, stream>>>((const char*)d_in[0], (const char*)d_in[1], (const char*)d_in[2], (const char*)d_in[4],
                                            (const char*)d_in[5], (const char*)d_in[6], (char*)d_out, (char*)d_ws);
}
```

```cpp
#include <hip/hip_runtime.h>
#include <stdint.h>
#include <utility>

#define GAS __attribute__((address_space(1)))
#define LAS __attribute__((address_space(3)))
#define AI __attribute__((always_inline))
typedef unsigned v4u __attribute__((ext_vector_type(4)));
typedef short v4s __attribute__((ext_vector_type(4)));
typedef short bf16x8 __attribute__((ext_vector_type(8)));
typedef float f32x4 __attribute__((ext_vector_type(4)));
typedef float f32x16 __attribute__((ext_vector_type(16)));
typedef float v2f __attribute__((ext_vector_type(2)));
typedef __bf16 v2bf __attribute__((ext_vector_type(2)));
typedef unsigned long long u64;

namespace {
constexpr unsigned FSTR = 4224;
constexpr unsigned WS_F1 = 65536, WS_FE = WS_F1 + 16 * FSTR, WS_F2 = WS_FE + 16 * FSTR;
constexpr unsigned WS_ST = 32768;
constexpr unsigned WS_QKV = 393216, QKV_T = 168 * 512;
constexpr unsigned WS_HO = WS_QKV + 64 * QKV_T, HO_T = 288 * 512;
constexpr unsigned WS_END = WS_HO + 64 * HO_T;
constexpr unsigned WS_DBG = 32u << 20;
constexpr u64 TAG_STEP = 0x9E3779B97F4A7C15ull;
constexpr int AUX_SC1 = 16;
constexpr unsigned SLOT = 40960, UNIT = 16384;
constexpr unsigned L_CSA = 122880, L_CNT = L_CSA + 3072;
constexpr unsigned CODE_BYTES = 19968;
constexpr unsigned OOB = 0x40000000u;
constexpr unsigned bunit(int s) { return (s == 0 ? 8 : s == 1 ? 9 : s == 2 ? 0 : s == 3 ? 1 : s == 4 ? 3 : s == 5 ? 4 : 5) * UNIT; }
constexpr unsigned wunit(int i) {
  return i == 18 ? 7 * UNIT : (i % 9) == 0 ? 2 * UNIT : (i % 9) == 1 ? 6 * UNIT : bunit(i % 9 - 2);
}
#define NPRE 1
constexpr int PRO = 10, G_ST = 17;
constexpr int avail(int g) {
  return g < 2 ? 20 : g < 6 ? 25 : g < 10 ? 30 : g < 14 ? 35 : g < 18 ? 39 : g < 22 ? 43 : g < 26 ? 49 : 53;
}
constexpr int cum(int g) {
  int c = PRO;
  for (int k = 0; k <= g; ++k) {
    int n = c + ((k & 1) && k != 27 ? 1 : 2);
    c = n < avail(k) ? n : avail(k);
  }
  return c;
}
static_assert(cum(27) == 53, "every item issued");
constexpr int nextra(int g) { return (g == 1) + (g == 7) + (g == G_ST); }
constexpr int nops(int g) {
  int c = PRO + 3;
  for (int k = 0; k <= g; ++k) c += cum(k) - cum(k - 1) + nextra(k);
  return c;
}
constexpr int rankI(int it) {
  if (it < PRO) return it < 5 ? it + 1 : it + 3;
  int g = 0;
  while (cum(g) <= it) ++g;
  return nops(g - 1) + it - cum(g - 1);
}
__device__ __forceinline__ unsigned umin(unsigned a, unsigned b) { return a < b ? a : b; }
constexpr int imin(int a, int b) { return a < b ? a : b; }
constexpr int U_F1 = 28, U_FE = 44, U_POLL = 38, U_ATT = 44;
#define PLAN 2
constexpr int bw2(int u) {
  return u == 8 ? 4 : u == 9 ? 5 : u == 10 ? 36 : u == 14 ? 6 : u == 16 ? 7 : u == 29 ? 18 : u == 30 ? 19 : u == 32 ? 8 : u == 33 ? 9
       : u == 35 ? 10 : u == 36 ? 11 : u == 39 ? 12 : u == 40 ? 13 : u == 42 ? 20 : u == 43 ? 21 : u == 48 ? 14 : u == 50 ? 15
       : u == 54 ? 16 : u == 55 ? 17 : -1;
}
constexpr int WREST = PLAN == 2 ? 11 : 13;
constexpr int breg(int u) { return u == 5 ? 0 : u == 13 ? 1 : u == 36 ? 2 : u == 44 ? 3 : u == 52 ? 4 : -1; }
constexpr int bops(int u) {
  const int p = bw2(u) >> 1;
  return (u == 1 || u == 3 || u == 11 || u == 19) + (bw2(u) >= 0 && p != 18) + (u == U_ATT || u == U_ATT + 1 ? 2 : u == U_ATT + 2 ? 1 : 0);
}
constexpr int bsum(int u0, int u1) { int c = 0; for (int u = u0; u < u1; ++u) c += bops(u); return c; }
constexpr int bslot(int piece) { int u = 0; while (bw2(u) != piece) ++u; return u; }

enum { T_Q, T_H, T_E, T_S, T_AQ, T_K0, T_K1, T_V0, T_V1, T_DA, T_DB, T_D18, T_F0, T_F1, T_ME, T_FLAGS,
       T_A, T_B, T_OUT, T_F2, T_NEED, T_O, T_STP, T_CS, T_WM, T_WMN, T_WMX, T_W2N, T_AWB, T_N = 32 };
enum { B_O, B_H, B_18, B_U12, B_7, B_6 };
struct Tab {
  unsigned v[2048][T_N];
  constexpr Tab() : v() {
    for (unsigned b = 0; b < 256; ++b)
      for (unsigned w = 0; w < 8; ++w) {
        unsigned* t = v[b * 8 + w];
        const unsigned xl = b & 7, idx = b >> 3, rg = w & 3, set = w >> 2;
        const bool typeA = (idx & 3) < 3, p2 = idx < 28, ow = w >= 6;
        const unsigned j = typeA ? 3 * (xl & 3) + (idx & 3) : 12 + (xl & 3), p = 8 * (xl >> 2) + (idx >> 2);
        const unsigned qb0 = typeA ? j : j == 15 ? 18 : 3 * j - 24, tt1 = 4 * p + rg;
        const unsigned rg2 = p2 ? 4 * xl + idx / 7 : 2 * p, cg = idx % 7, kL = w < 5 ? 11 * w : 54;
        t[T_Q] = j < 15 ? WS_QKV + tt1 * QKV_T + (2 * qb0 + set) * 2048 : OOB;
        t[T_H] = WS_HO + tt1 * HO_T + (2 * j + set) * 2048;
        t[T_E] = typeA ? t[T_H] : OOB;
        t[T_S] = WS_ST + tt1 * 512;
        const unsigned aq = WS_QKV + (4 * p + (w >> 1)) * QKV_T + (w & 1) * 256 + ((7 * j) >> 1) * 512;
        const unsigned ak = aq - ((w & 1) ? 256u : (p | w) ? QKV_T - 256u : 0u);
        t[T_AQ] = aq; t[T_K0] = ak + 56 * 512; t[T_K1] = aq + 56 * 512; t[T_V0] = ak + 112 * 512; t[T_V1] = aq + 112 * 512;
        t[T_O] = WS_HO + (4 * p + (w >> 1)) * HO_T + (w & 1) * 256 + (224 + 4 * j) * 512;
        t[T_DA] = p2 ? cg * 128 + (ow ? (w - 6) * 224 + 112 : 128 * w) * 896 : OOB;
        t[T_DB] = p2 ? cg * 128 + (ow ? (w - 6) * 224 : 768 + 16 * kL) * 896 : OOB;
        t[T_D18] = ow ? 140 * 896 : 160 * 896;
        t[T_STP] = ow ? 4 * 896 + 1024 : 8 * 896 + 1024;
        t[T_W2N] = ow ? 448 * 896 : 1792 * 896;
        t[T_A] = WS_HO + 2 * rg2 * HO_T + (ow ? 120 + 16 * (w - 6) : 8 * w) * 1024;
        t[T_B] = WS_HO + 2 * rg2 * HO_T + (ow ? 112 + 16 * (w - 6) : 48 + kL) * 1024;
        t[T_OUT] = (64 * rg2 + 8 * w) * 896 + cg * 128;
        t[T_F0] = w == 6 ? WS_FE + (rg2 >> 1) * FSTR : WS_F1 + p * FSTR;
        t[T_F1] = w != 6 && p > 0 ? FSTR : 0;
        t[T_F2] = WS_F2 + (rg2 >> 1) * FSTR;
        t[T_ME] = WS_F1 + p * FSTR + j * 8;
        t[T_NEED] = ow ? 0xffu << (8 * (w - 6)) : ((2u << ((kL + (w < 4 ? 10 : 9)) >> 2)) - 1) & ~((1u << (kL >> 2)) - 1);
        const bool c18 = w < 4 || ow;
        t[T_FLAGS] = ow << B_O | (p2 && !ow) << B_H | c18 << B_18 | (c18 || !typeA) << B_U12 | (w == 7) << B_7 | (w == 6) << B_6;
        t[T_CS] = L_CSA + set * 1536;
        t[T_AWB] = 8 * UNIT - 2 * (rg ^ (2 * set)) * 1024;
        const unsigned n = w < 4 ? 14 : w < 7 ? 17 : 13;
        t[T_WM] = (w < 4 ? b * 2 + (w & 1) : w < 7 ? b * 3 + w - 4 : b) * n; t[T_WMN] = n - 1; t[T_WMX] = w < 7 ? 12543u : 3135u;
      }
  }
};
__constant__ const Tab kTab = Tab();
typedef unsigned v16u __attribute__((ext_vector_type(16)));

template <int N> using IC = std::integral_constant<int, N>;
template <int... I, class F> __device__ __forceinline__ void sfor_(std::integer_sequence<int, I...>, F&& f) { (f(std::integral_constant<int, I>{}), ...); }
template <int N, class F> __device__ __forceinline__ void sfor(F&& f) { sfor_(std::make_integer_sequence<int, N>{}, f); }

#define WAITL0() asm volatile("s_waitcnt lgkmcnt(0)" ::: "memory")
#define BAR() do { asm volatile("" ::: "memory"); __builtin_amdgcn_s_barrier(); asm volatile("" ::: "memory"); } while (0)
#define SCHED() __builtin_amdgcn_sched_barrier(0)
#define PRIO(n) __builtin_amdgcn_s_setprio(n)
template <class T> __device__ __forceinline__ void TIE(T& v) { asm volatile("" : "+v"(v)); }
template <class T> __device__ __forceinline__ void TIES(T& v) { asm volatile("" : "+s"(v)); }
template <int N> __device__ __forceinline__ void waitv() { asm volatile("s_waitcnt vmcnt(%0)" ::"i"(N) : "memory"); }
template <int N> __device__ __forceinline__ void waitl() { __builtin_amdgcn_s_waitcnt(0xC07F | (N << 8)); }

typedef __amdgpu_buffer_rsrc_t rsrc_t;
__device__ __forceinline__ rsrc_t mkrs(const char* p, unsigned bytes) {
  rsrc_t r = __builtin_amdgcn_make_buffer_rsrc((void*)p, 0, bytes, 0x00020000);
  TIES(r);
  return r;
}
template <unsigned L> __device__ __forceinline__ void setm0(unsigned ldsw) {
  asm volatile("s_add_u32 m0, %0, %1\n\ts_nop 0" ::"s"(ldsw), "i"(L) : "memory", "scc");
}
template <unsigned O> __device__ __forceinline__ void dmag(unsigned voff, const char* base) {
  asm volatile("global_load_lds_dwordx4 %0, %1 offset:%2" ::"v"(voff), "s"(base), "i"(O) : "memory");
}
template <unsigned O> __device__ __forceinline__ void dmab(unsigned voff, rsrc_t rs) {
  asm volatile("buffer_load_dwordx4 %0, %1, 0 offen offset:%2 lds" ::"v"(voff), "s"(rs), "i"(O) : "memory");
}
template <unsigned L, int B> __device__ __forceinline__ void dmab2c(unsigned v0, unsigned v1, rsrc_t rs, unsigned ldsw, unsigned c) {
  asm volatile("s_bitcmp0_b32 %5, %6\n\ts_cbranch_scc1 1f\n\ts_add_u32 m0, %3, %4\n\ts_nop 0\n\tbuffer_load_dwordx4 %0, %2, 0 offen lds\n\t"
               "buffer_load_dwordx4 %1, %2, 0 offen offset:1024 lds\n1:" ::"v"(v0), "v"(v1), "s"(rs), "s"(ldsw), "i"(L), "s"(c), "i"(B) : "memory", "scc");
}
__device__ __forceinline__ void ld16(v4u& d, unsigned voff, rsrc_t rs) {
  asm volatile("buffer_load_dwordx4 %0, %1, %2, 0 offen sc1" : "=v"(d) : "v"(voff), "s"(rs) : "memory");
}
__device__ __forceinline__ void ld16s(v4u& d, unsigned voff, rsrc_t rs, unsigned so) {
  asm volatile("buffer_load_dwordx4 %0, %1, %2, %3 offen sc1" : "=v"(d) : "v"(voff), "s"(rs), "s"(so) : "memory");
}
template <unsigned O> __device__ __forceinline__ void ld16x2(v4u& d0, v4u& d1, unsigned v, rsrc_t rs, unsigned so) {
  asm volatile("buffer_load_dwordx4 %0, %2, %3, 0 offen offset:%5 sc1\n\tbuffer_load_dwordx4 %1, %2, %3, %4 offen offset:%5 sc1"
               : "=&v"(d0), "=&v"(d1) : "v"(v), "s"(rs), "s"(so), "i"(O) : "memory");
}
template <unsigned O, int B> __device__ __forceinline__ void ld16x2c(v4u& d0, v4u& d1, unsigned v, rsrc_t rs, unsigned so, unsigned c) {
  asm volatile("s_bitcmp0_b32 %6, %7\n\ts_cbranch_scc1 1f\n\tbuffer_load_dwordx4 %0, %2, %3, 0 offen offset:%5 sc1\n\t"
               "buffer_load_dwordx4 %1, %2, %3, %4 offen offset:%5 sc1\n1:"
               : "=&v"(d0), "=&v"(d1) : "v"(v), "s"(rs), "s"(so), "i"(O), "s"(c), "i"(B) : "memory", "scc");
}
template <unsigned O, int B> __device__ __forceinline__ void ld16x4c(v4u& d0, v4u& d1, v4u& d2, v4u& d3, unsigned v, rsrc_t rs, unsigned so, unsigned c) {
  asm volatile("s_bitcmp0_b32 %9, %10\n\ts_cbranch_scc1 1f\n\tbuffer_load_dwordx4 %0, %4, %5, 0 offen offset:%7 sc1\n\t"
               "buffer_load_dwordx4 %1, %4, %5, %6 offen offset:%7 sc1\n\tbuffer_load_dwordx4 %2, %4, %5, 0 offen offset:%8 sc1\n\t"
               "buffer_load_dwordx4 %3, %4, %5, %6 offen offset:%8 sc1\n1:"
               : "=&v"(d0), "=&v"(d1), "=&v"(d2), "=&v"(d3) : "v"(v), "s"(rs), "s"(so), "i"(O), "i"(O + 1024), "s"(c), "i"(B) : "memory", "scc");
}
__device__ __forceinline__ void ld16x10r(v4u* a, v4u* b, v4u* c_, v4u* d, v4u* e, unsigned v0, unsigned v1, rsrc_t rs, unsigned so, unsigned c) {
  asm volatile("s_bitcmp0_b32 %14, 0\n\ts_cbranch_scc1 1f\n\t"
               "buffer_load_dwordx4 %0, %10, %12, 0 offen offset:3072 sc1\n\tbuffer_load_dwordx4 %1, %10, %12, %13 offen offset:3072 sc1\n\t"
               "buffer_load_dwordx4 %2, %11, %12, 0 offen sc1\n\tbuffer_load_dwordx4 %3, %11, %12, %13 offen sc1\n\t"
               "buffer_load_dwordx4 %4, %11, %12, 0 offen offset:1024 sc1\n\tbuffer_load_dwordx4 %5, %11, %12, %13 offen offset:1024 sc1\n\t"
               "buffer_load_dwordx4 %6, %11, %12, 0 offen offset:2048 sc1\n\tbuffer_load_dwordx4 %7, %11, %12, %13 offen offset:2048 sc1\n\t"
               "buffer_load_dwordx4 %8, %11, %12, 0 offen offset:3072 sc1\n\tbuffer_load_dwordx4 %9, %11, %12, %13 offen offset:3072 sc1\n1:"
               : "+v"(a[0]), "+v"(a[1]), "+v"(b[0]), "+v"(b[1]), "+v"(c_[0]), "+v"(c_[1]), "+v"(d[0]), "+v"(d[1]), "+v"(e[0]), "+v"(e[1])
               : "v"(v0), "v"(v1), "s"(rs), "s"(so), "s"(c) : "memory", "scc");
}
__device__ __forceinline__ void touch(unsigned& d, unsigned voff, const char* base) {
  asm volatile("global_load_dword %0, %1, %2" : "=v"(d) : "v"(voff), "s"(base) : "memory");
}
template <int N1, int N0, int B> __device__ __forceinline__ void waitc(unsigned c) {
  static_assert(N0 <= N1 && N0 >= 0, "");
  asm volatile("s_bitcmp1_b32 %0, %3\n\ts_cbranch_scc1 1f\n\ts_waitcnt vmcnt(%2)\n1:\n\ts_waitcnt vmcnt(%1)" ::"s"(c), "i"(N1), "i"(N0), "i"(B) : "memory", "scc");
}
__device__ __forceinline__ float wave_sum63(float v) {
#define DPP_ADD(ctrl, rm) v += __builtin_bit_cast(float, __builtin_amdgcn_update_dpp(0, __builtin_bit_cast(int, v), ctrl, rm, 0xf, true));
  DPP_ADD(0x111, 0xf) DPP_ADD(0x112, 0xf) DPP_ADD(0x114, 0xf) DPP_ADD(0x118, 0xf) DPP_ADD(0x142, 0xa) DPP_ADD(0x143, 0xc)
#undef DPP_ADD
  return v;
}
__device__ __forceinline__ bf16x8 ldtr(unsigned a) {
  v4s lo = __builtin_amdgcn_ds_read_tr16_b64_v4i16((LAS v4s*)(uintptr_t)a);
  v4s hi = __builtin_amdgcn_ds_read_tr16_b64_v4i16((LAS v4s*)(uintptr_t)(a + 512));
  return __builtin_shufflevector(lo, hi, 0, 1, 2, 3, 4, 5, 6, 7);
}
__device__ __forceinline__ unsigned pk(float a, float b) {
  v2f f = {a, b};
  return __builtin_bit_cast(unsigned, __builtin_convertvector(f, v2bf));
}
__device__ __forceinline__ float asf(unsigned d) { return __builtin_bit_cast(float, d); }
__device__ __forceinline__ unsigned asu(float f) { return __builtin_bit_cast(unsigned, f); }
__device__ __forceinline__ float blo(unsigned d) { return __builtin_bit_cast(float, d << 16); }
__device__ __forceinline__ float bhi(unsigned d) { return __builtin_bit_cast(float, d & 0xffff0000u); }
__device__ __forceinline__ f32x16 mm32(bf16x8 a, bf16x8 b, f32x16 c) { return __builtin_amdgcn_mfma_f32_32x32x16_bf16(a, b, c, 0, 0, 0); }
__device__ __forceinline__ f32x4 mm16(bf16x8 a, bf16x8 b, f32x4 c) { return __builtin_amdgcn_mfma_f32_16x16x32_bf16(a, b, c, 0, 0, 0); }
__device__ __forceinline__ bf16x8 asb(v4u v) { return __builtin_bit_cast(bf16x8, v); }
template <bool MAX> __device__ __forceinline__ float rows4(float v) {
  {
    const auto r = __builtin_amdgcn_permlane16_swap(asu(v), asu(v), false, false);
    const unsigned a = r[0], b = r[1];
    v = MAX ? __builtin_fmaxf(asf(a), asf(b)) : asf(a) + asf(b);
  }
  const auto r = __builtin_amdgcn_permlane32_swap(asu(v), asu(v), false, false);
  const unsigned a = r[0], b = r[1];
  return MAX ? __builtin_fmaxf(asf(a), asf(b)) : asf(a) + asf(b);
}
template <int B> __device__ __forceinline__ void raise(unsigned foff, rsrc_t rs, u64 tv, unsigned c) {
  asm volatile("s_bitcmp0_b32 %3, %4\n\ts_cbranch_scc1 1f\n\ts_mov_b64 exec, 1\n\tbuffer_store_dwordx2 %0, off, %1, %2 sc1\n\ts_mov_b64 exec, -1\n1:"
               ::"v"(tv), "s"(rs), "s"(foff), "s"(c), "i"(B) : "memory", "scc");
}
__device__ __forceinline__ void arrive(unsigned cnt, unsigned foff, rsrc_t rs, u64 tv) {
  unsigned a = cnt, t;
  asm volatile("s_mov_b64 exec, 1\n\tv_mov_b32 %0, 1\n\tds_add_rtn_u32 %0, %1, %0\n\ts_waitcnt lgkmcnt(0)\n\t"
               "v_cmp_eq_u32 vcc, 7, %0\n\ts_cbranch_vccz 1f\n\tbuffer_store_dwordx2 %2, off, %3, %4 sc1\n1:\n\ts_mov_b64 exec, -1"
               : "=&v"(t) : "v"(a), "v"(tv), "s"(rs), "s"(foff) : "memory", "vcc");
}
template <int B> __device__ __forceinline__ void ld8c(u64& d, unsigned voff, rsrc_t rs, unsigned c) {
  asm volatile("s_bitcmp0_b32 %3, %4\n\ts_cbranch_scc1 1f\n\tbuffer_load_dwordx2 %0, %1, %2, 0 offen sc1\n1:" : "=&v"(d) : "v"(voff), "s"(rs), "s"(c), "i"(B) : "memory", "scc");
}
template <int N, int B> __device__ __forceinline__ void poll(u64 v, unsigned voff, rsrc_t rs, u64 tag, unsigned c) {
  unsigned n = 1u << 19;
  asm volatile("s_bitcmp0_b32 %5, %7\n\ts_cbranch_scc1 2f\n\ts_waitcnt vmcnt(%6)\n1:\n\tv_cmp_ne_u64 vcc, %4, %0\n\ts_cbranch_vccz 2f\n\ts_sleep 3\n\t"
               "buffer_load_dwordx2 %0, %2, %3, 0 offen sc1\n\ts_sub_u32 %1, %1, 1\n\ts_waitcnt vmcnt(0)\n\ts_cbranch_scc0 1b\n2:"
               : "+v"(v), "+s"(n) : "v"(voff), "s"(rs), "s"(tag), "s"(c), "i"(N), "i"(B) : "memory", "vcc", "scc");
}
__device__ __forceinline__ void watch(unsigned voff, rsrc_t rs, u64 tag, unsigned lds, unsigned need) {
  unsigned n = 1u << 19, a = lds, t, m;
  u64 v;
  asm volatile("1:\n\tbuffer_load_dwordx2 %0, %4, %5, 0 offen sc1\n\ts_waitcnt vmcnt(0)\n\tv_cmp_eq_u64 vcc, %6, %0\n\ts_mov_b32 %3, vcc_lo\n\t"
               "v_mov_b32 %2, %3\n\ts_mov_b64 exec, 1\n\tds_or_b32 %7, %2\n\ts_mov_b64 exec, -1\n\t"
               "s_andn2_b32 %3, %8, %3\n\ts_cbranch_scc0 2f\n\ts_sleep 1\n\ts_sub_u32 %1, %1, 1\n\ts_cbranch_scc0 1b\n2:"
               : "=&v"(v), "+s"(n), "=&v"(t), "=&s"(m) : "v"(voff), "s"(rs), "s"(tag), "v"(a), "s"(need) : "memory", "vcc", "scc");
}
__device__ __forceinline__ void waitw(unsigned lds, unsigned need) {
  unsigned a = lds, t, m, n = 1u << 22;
  asm volatile("1:\n\tds_read_b32 %0, %3\n\ts_waitcnt lgkmcnt(0)\n\tv_readfirstlane_b32 %2, %0\n\ts_andn2_b32 %2, %4, %2\n\ts_cbranch_scc0 2f\n\t"
               "s_sleep 1\n\ts_sub_u32 %1, %1, 1\n\ts_cbranch_scc0 1b\n2:"
               : "=&v"(t), "+s"(n), "=&s"(m) : "v"(a), "s"(need) : "memory", "scc");
}
__device__ __forceinline__ void pollg(v4u& v, unsigned voff, rsrc_t rs, u64 tag) {
  unsigned v1 = v[1], v3 = v[3];
  const unsigned lo = (unsigned)tag, hi = (unsigned)(tag >> 32);
  if (__builtin_amdgcn_ballot_w64(((v1 ^ lo) | (v3 ^ hi)) != 0) == 0) return;
#pragma unroll 1
  for (unsigned n = 1u << 20; n; --n) {
    v = __builtin_amdgcn_raw_buffer_load_b128(rs, voff, 0, AUX_SC1);
    v1 = v[1]; v3 = v[3];
    if (__builtin_amdgcn_ballot_w64(((v1 ^ lo) | (v3 ^ hi)) != 0) == 0) break;
    __builtin_amdgcn_s_sleep(1);
  }
}
__device__ __forceinline__ bf16x8 csfrag(f32x4 p) {
  const float c = (p[0] + p[1]) + (p[2] + p[3]);
  const unsigned h = pk(c, 0.f);
  const unsigned d = pk(c, c - blo(h));
  return asb(v4u{d, d, 0u, 0u});
}
__device__ __forceinline__ float silu_k(float g, float k1, float ik2) {
  return g * __builtin_amdgcn_rcpf(__builtin_fmaf(__builtin_amdgcn_exp2f(g * k1), ik2, ik2));
}
#define STAMP(i)
#define CSTAMP(i)
#define STAMPB(i)
#define STAMPP(i)
#define STAMPN(i) STAMP(i)
}

#if defined(__HIP_DEVICE_COMPILE__)
__attribute__((target("no-packed-fp32-ops")))
#endif
__global__ __launch_bounds__(512, 2) void fused_block(const char* __restrict__ x, const char* __restrict__ wqkv, const char* __restrict__ wo,
                                                      const char* __restrict__ wg, const char* __restrict__ wu, const char* __restrict__ wd,
                                                      char* __restrict__ out, char* __restrict__ ws) {
  STAMP(0);
  const unsigned tid = threadIdx.x, lane = tid & 63, wv = tid >> 6, b = blockIdx.x, xl = b & 7, idx = b >> 3;
  unsigned warm0, warm, warm2;
  {
    u64 pc;
    asm volatile("s_getpc_b64 %0" : "=s"(pc));
    touch(warm0, umin((((b & 56u) + wv) * 3 + (tid & 3)) * 128, CODE_BYTES - 128), (const char*)pc);
  }
  asm volatile("" ::"s"(x), "s"(wqkv), "s"(wo), "s"(wg), "s"(wu), "s"(wd), "s"(out), "s"(ws));
  const unsigned w = __builtin_amdgcn_readfirstlane(wv);
  v16u ta, tb;
  asm volatile("s_load_dwordx16 %0, %2, 0x0\n\ts_load_dwordx16 %1, %2, 0x40" : "=&s"(ta), "=&s"(tb) : "s"(kTab.v[b * 8 + w]) : "memory");
#define TV(i) ((i) < 16 ? ta[(i) & 15] : tb[(i) & 15])
  const bool typeA = (idx & 3) < 3;
  const unsigned j = typeA ? 3 * (xl & 3) + (idx & 3) : 12 + (xl & 3), p = 8 * (xl >> 2) + (idx >> 2);
  const unsigned lw2 = w * 2048, lw3 = w * 3072;

  STAMPP(5);
  const unsigned l3 = lane >> 3, l7 = lane & 7, r8 = tid >> 3;
  const unsigned cw = (l7 ^ ((lane >> 2) & 4)) * 16;
  const unsigned vx0 = (128 * p + 8 * wv + r8) * 896 + ((l7 ^ (lane >> 4)) * 16), vx1 = (vx0 ^ 64) + (8 * 896 - 1024);
  const unsigned qb0 = typeA ? j : umin(3 * j - 24, 18u);
  unsigned vq = r8 * 2688 + qb0 * 128 + cw, vf = r8 * 3584 + j * 128 + cw, v12 = typeA ? vf : vq;
  const char* b1 = (typeA ? wg : wqkv + 128) - 1024;
  const char* b2 = (typeA ? wu : wqkv + 256) - 2048;
  const unsigned st12 = typeA ? 64 * 3584 : 64 * 2688;
  auto itemA = [&](auto it_) AI {
    constexpr int it = it_, s = it / 5, k = it % 5;
    constexpr unsigned sl = (s & 3) * SLOT;
    if constexpr (k == 0) { setm0<sl + 24576 - s * 128>(lw2); dmag<s * 128>(vx0, x); }
    else if constexpr (k == 1) dmag<s * 128 + 1024>(vx1, x);
    else if constexpr (k == 2) { setm0<sl>(lw3); dmag<0>(vq, wqkv); vq += 64 * 2688; TIE(vq); }
    else if constexpr (k == 3) dmag<1024>(v12, b1);
    else { dmag<2048>(v12, b2); v12 += st12; TIE(v12); }
  };
  sfor<5>([&](auto it) AI { itemA(it); });
  BAR();
  const unsigned ts = 128 * p + 8 * j + wv;
  v4u xrow;
  asm volatile("global_load_dwordx4 %0, %1, %2" : "=v"(xrow) : "v"(ts * 896 + umin(lane, 55u) * 16), "s"(x) : "memory");
  u64 tag;
  asm volatile("s_load_dwordx2 %0, %1, 0x0" : "=s"(tag) : "s"(ws) : "memory");
  STAMPP(6);
  {
    const unsigned n = wv < 2 ? 19 : 7, k = wv < 2 ? wv : wv < 5 ? wv - 2 : wv - 5;
    const unsigned L = umin((b * (wv < 2 ? 2 : 3) + k) * n + umin(lane, n - 1), 9407u);
    touch(warm, L * 128 + (wv < 2 ? 0u : ((L * 43691u) >> 19) * 2048), w < 2 ? wqkv : w < 5 ? wg : wu);
  }
  SCHED();
  STAMPP(7);
  const unsigned rg = wv & 3, set = wv >> 2, rr = rg ^ (2 * set), tok = lane & 31, hh = lane >> 5;
  const unsigned q4 = (lane & 15) >> 2, p4 = lane & 3, sp4 = ((p4 & 1) << 1) | (p4 >> 1);
  const unsigned trl = q4 * 128 + (((lane >> 2) & 4) + sp4) * 8;
  const unsigned aw = trl + ((set ^ (q4 >> 1)) << 6);
  const unsigned ax = (32 * rg + tok) * 128 + (((2 * rr + hh) ^ ((tok >> 1) & 7)) << 4);
  const unsigned selw = (((lane >> 4) ^ (lane >> 2)) & 1) ? 0u : 0x3F803F80u;
  const bf16x8 sel = asb(v4u{selw, selw, selw, selw});
  unsigned bw[4], bx[4];
#pragma unroll
  for (int i = 0; i < 4; ++i) { bw[i] = aw + (2 * (rr ^ i) + hh) * 3072; bx[i] = (ax ^ (i * 32)) + 24576; TIE(bw[i]); TIE(bx[i]); }

  const rsrc_t rs = mkrs(ws, WS_END);
  unsigned vwm;
  const char* bwm = w < 2 ? wg : w < 4 ? wu : w < 7 ? wd : wo;
  const bool p2 = idx < 28, owave = w >= 6;
  const char* w2p = owave ? wo : wd;
  TIES(w2p);
  unsigned vdA, vdB, vdC, awB, a2, vl, oS, aC, la, fo1;
  rsrc_t rw2;
  auto setup = [&](auto n_) AI {
    constexpr int n = n_;
    unsigned l = lane;
    TIE(l);
    if constexpr (n == 4) {
      awB = bw[0] - (l >> 5) * 1024 + TV(T_AWB);
      a2 = ((bw[0] & 1023u) ^ ((w & 4) << 4)) + (l >> 5) * 1024 + lw2;
      TIE(awB); TIE(a2);
    } else if constexpr (n == 5) {
      oS = (l & 31) * 16 + TV(T_S); aC = (l & 31) * 16 + TV(T_CS); vl = l * 16;
      TIE(oS); TIE(aC); TIE(vl);
    } else if constexpr (n == 7) {
      la = (l & 15) * 16 + (l >> 4) * 512;
      TIE(la);
    } else if constexpr (n == 8) {
      vdA = (l >> 3) * 896 + ((l & 7) ^ ((l >> 2) & 4)) * 16;
      TIE(vdA);
    } else if constexpr (n == 9) {
      vdB = vdA + TV(T_DB); vdC = vdB + 16 * 896; vdA += TV(T_DA);
      TIE(vdA); TIE(vdB); TIE(vdC);
    } else if constexpr (n == 11) {
      fo1 = TV(T_F0) + (l & 15) * 8 - ((l >> 4) & 1) * TV(T_F1);
      TIE(fo1);
    } else if constexpr (n == 12) {
      rw2 = mkrs(w2p, TV(T_W2N));
    }
  };
  auto issueWh = [&](auto i_, auto hf_) AI {
    constexpr int i = i_, hf = hf_;
    unsigned& v = i < 8 ? vdA : i == 8 ? vdB : vdC;
    if constexpr (i == 18) { const unsigned v18 = vdB + TV(T_D18); dmab2c<wunit(i), B_18>(v18, v18 + (8 * 896 - 1024), rw2, lw2, TV(T_FLAGS)); }
    else if constexpr (hf == 0) { setm0<wunit(i)>(lw2); dmab<0>(v, rw2); v += 8 * 896 - 1024; TIE(v); }
    else { dmab<1024>(v, rw2); v += (i & 1) ? TV(T_STP) : 8 * 896 + 1024; TIE(v); }
  };
  auto issueW = [&](auto i) AI { issueWh(i, IC<0>{}); issueWh(i, IC<1>{}); };
  vf += 1536;
  auto item = [&](auto it_) AI {
    constexpr int it = it_;
    if constexpr (it < 35) itemA(it_);
    else if constexpr (it == 47 || it == 48 || it >= 51) issueWh(IC<(it >= 51)>{}, IC<((it - 47) & 1)>{});
    else {
      constexpr int h = it < 47 ? it - 35 : it - 37;
      if constexpr (!(h & 1)) setm0<bunit(h >> 1)>(lw2);
      dmag<(h & 1) * 1024>(vf, h >= 7 ? wu : wg);
      vf += (h == 6 ? -6 * 64 * 3584 : 64 * 3584) + ((h & 1) ? 1024 : -1024);
      TIE(vf);
    }
  };
  const unsigned odd = j & 1;

  bf16x8 xf[28];
  f32x16 acc[3], accB[2];
  f32x4 cs[3], csB[2], cp[3];
#pragma unroll
  for (int t = 0; t < 3; ++t) { acc[t] = 0.f; cs[t] = 0.f; }
#pragma unroll
  for (int t = 0; t < 2; ++t) { accB[t] = 0.f; csB[t] = 0.f; }
  bf16x8 wf[3][3], wb[6];
  auto rdX = [&](auto n_) AI {
    constexpr int n = n_, s = n >> 2, i = n & 3;
    if constexpr (s == 2 || s == 6) { bx[i] += 2 * SLOT; TIE(bx[i]); }
    if constexpr (s == 4) { bx[i] -= 2 * SLOT; TIE(bx[i]); }
    xf[n] = *(const LAS bf16x8*)(uintptr_t)(bx[i] + (s & 1) * SLOT);
  };
  auto rdW = [&](auto n_, auto t_) AI {
    constexpr int n = n_, t = t_, s = n >> 2, i = n & 3;
    if constexpr (t == 0 && (s == 2 || s == 6)) { bw[i] += 2 * SLOT; TIE(bw[i]); }
    if constexpr (t == 0 && s == 4) { bw[i] -= 2 * SLOT; TIE(bw[i]); }
    wf[n % 3][t] = ldtr(bw[i] + (s & 1) * SLOT + t * 1024);
  };
  auto rdB = [&](auto u_) AI {
    constexpr int u = u_, i = u & 3, h = u >> 2;
    constexpr unsigned base = h < 4 ? 8 * UNIT : h < 10 ? 0 : 4 * UNIT;
    if constexpr (h == 0) { bw[i] = awB ^ (i * 4096); TIE(bw[i]); }
    if constexpr (h == 4) { bw[i] -= 8 * UNIT; TIE(bw[i]); }
    if constexpr (h == 10) { bw[i] += 4 * UNIT; TIE(bw[i]); }
    constexpr unsigned off = bunit(h >> 1) - base + (h & 1) * 1024;
    wb[u % 6] = ldtr(bw[i] + off);
  };
  v4u stg;
  float rstd, sa = 0.f, sq = 0.f;
  bf16x8 mu, cf[3];

  asm volatile("s_waitcnt lgkmcnt(0)" : "+s"(tag), "+s"(ta), "+s"(tb)::"memory");
  {
    const unsigned L = umin(TV(T_WM) + umin(lane, TV(T_WMN)), TV(T_WMX));
    vwm = L * 128 + (wv < 4 ? (L >> 4) * 1536 + 1536 : 0u);
    TIE(vwm);
  }
  tag += TAG_STEP;
  u64 tagv = tag;
  TIE(tagv);
  STAMPP(8);
  sfor<PRO - 5>([&](auto it) AI { itemA(IC<5 + it>{}); });
  STAMPP(9);
  waitv<PRO - 5 + 2>();
  TIE(warm0);
  BAR();
  STAMP(1); CSTAMP(12);
  sfor<2>([&](auto n) AI { rdX(n); rdW(n, IC<0>{}); rdW(n, IC<1>{}); rdW(n, IC<2>{}); });
  sfor<28>([&](auto n_) AI {
    constexpr int n = n_, s = n >> 2, i = n & 3, c0 = cum(n - 1), c1 = cum(n);
    SCHED();
    if constexpr (i != 2) PRIO(3 - ((i + 2) & 3));
    if constexpr (i != 3 && n > 0) waitl<7>();
    acc[0] = mm32(wf[n % 3][0], xf[n], acc[0]);
    SCHED();
    if constexpr (i == 2) {
      if constexpr (s == 4) { if (tid < 4) *(LAS unsigned*)(uintptr_t)(L_CNT + tid * 4) = 0u; }
      if constexpr (s == 6) {
#pragma unroll
        for (int t = 0; t < 3; ++t) *(LAS float*)(uintptr_t)(aC + (w & 3) * 4 + t * 512) = cs[t][0];
      }
      waitl<0>();
      static_assert(c0 > (s < 6 ? 5 * s + 9 : 36), "next stage issued");
      constexpr int y = nops(n - 1) - 1 - rankI(s < 6 ? 5 * s + 9 : 36);
      waitv<s == 5 ? imin(y, nops(n - 1) - nops(G_ST)) : y>();
      if constexpr (s == 0) { TIE(warm); TIE(xrow); }
      if constexpr (s == 1) { static_assert(rankI(14) >= nops(1), "covered"); TIE(warm2); }
#pragma unroll
      for (int t = 0; t < 3; ++t) { TIE(acc[t]); TIE(cs[t]); }
      BAR();
      PRIO(3);
      if constexpr (s == 6) {
#pragma unroll
        for (int t = 0; t < 3; ++t) cp[t] = *(const LAS f32x4*)(uintptr_t)(aC + t * 512);
      }
    }
    if constexpr (n < 26) {
      rdX(IC<n + 2>{});
      rdW(IC<n + 2>{}, IC<0>{});
      if constexpr (i == 1) rdW(IC<n + 2>{}, IC<1>{});
    } else rdB(IC<2 * (n - 26)>{});
    if constexpr (n == 2 || n == 3) {
#pragma unroll
      for (int e = 2 * (n - 2); e < 2 * (n - 2) + 2; ++e) {
        const unsigned d = lane < 56 ? xrow[e] : 0u;
        const float lo = blo(d), hi = bhi(d);
        sa += lo + hi; sq = __builtin_fmaf(lo, lo, __builtin_fmaf(hi, hi, sq));
      }
      TIE(sa); TIE(sq);
    }
    if constexpr (n == 6) { sa = wave_sum63(sa); sq = wave_sum63(sq); TIE(sa); TIE(sq); }
    if constexpr (n >= 4 && n <= 12 && i != 2) setup(n_);
    if constexpr (n == 23) {
      TIE(stg);
      pollg(stg, oS, rs, tag);
      rstd = asf(stg[2]);
      const float m = asf(stg[0]);
      const unsigned mh = pk(-m, -m);
      const float ml = -m - blo(mh);
      mu = asb(v4u{hh ? 0u : mh, hh ? 0u : pk(ml, ml), 0u, 0u});
      TIE(mu); TIE(rstd);
    }
    if constexpr (n == 27) { cf[1] = csfrag(cp[1]); TIE(cf[1]); }
    SCHED();
    if constexpr (i == 0) cs[0] = mm16(sel, wf[n % 3][0], cs[0]);
    if constexpr (n == 27) acc[0] = mm32(cf[0], mu, acc[0]);
    acc[1] = mm32(wf[n % 3][1], xf[n], acc[1]);
    SCHED();
    if constexpr (n < 26) rdW(IC<n + 2>{}, IC<(i == 1 ? 2 : 1)>{}); else rdB(IC<2 * (n - 26) + 1>{});
    if constexpr (c1 > c0) item(IC<c0>{});
    if constexpr (n == 27) { cf[2] = csfrag(cp[2]); TIE(cf[2]); }
    SCHED();
    if constexpr (i == 0) cs[1] = mm16(sel, wf[n % 3][1], cs[1]);
    if constexpr (n == 27) acc[1] = mm32(cf[1], mu, acc[1]);
    acc[2] = mm32(wf[n % 3][2], xf[n], acc[2]);
    SCHED();
    if constexpr (i != 1 && n < 26) rdW(IC<n + 2>{}, IC<2>{});
    if constexpr (c1 > c0 + 1) item(IC<c0 + 1>{});
    if constexpr (n == 1) touch(warm2, vwm, bwm);
    if constexpr (n == 7) {
      const float m = sa * (1.0f / 448.0f);
      const float r = __builtin_amdgcn_rsqf(__builtin_fmaxf(sq * (1.0f / 448.0f) - m * m, 0.f) + 1e-5f);
      const v4u gr = {asu(m), (unsigned)tagv, asu(r), (unsigned)(tagv >> 32)};
      __builtin_amdgcn_raw_buffer_store_b128(gr, rs, lane == 63 ? WS_ST + ts * 16 : OOB, 0, AUX_SC1);
    }
    if constexpr (n == G_ST) ld16(stg, oS, rs);
    if constexpr (n == 26) { cf[0] = csfrag(cp[0]); TIE(cf[0]); }
    SCHED();
    if constexpr (i == 0) cs[2] = mm16(sel, wf[n % 3][2], cs[2]);
    if constexpr (n == 27) acc[2] = mm32(cf[2], mu, acc[2]);
  });
  SCHED();
  STAMP(2); CSTAMP(13);
  u64 fl;
  v4u qf, k0, k1, v0, v1, hv;
  bf16x8 w2[9][2];
  float sg[16];
  const float k1s = -1.4426950408889634f * rstd, ik2 = __builtin_amdgcn_rcpf(rstd * rstd);
  auto rdW2 = [&](auto i_, bf16x8* d) AI {
    constexpr unsigned U = wunit(decltype(i_)::value);
    unsigned c = a2 + U, e = c ^ 64;
    TIE(c); TIE(e);
    d[0] = ldtr(c); d[1] = ldtr(e);
  };
  const unsigned oQ = vl + TV(T_Q), fl_ = TV(T_FLAGS), fme = TV(T_ME);
  unsigned hd[4];
  float hprev;
  sfor<56>([&](auto u_) AI {
    constexpr int u = u_, t = u >= 28, k = u % 28;
    SCHED();
    accB[t] = mm32(wb[u % 6], xf[k], accB[t]);
    SCHED();
    if constexpr ((u & 7) == 4) {
      if constexpr (u == 28 || u == 52) *(LAS float*)(uintptr_t)(aC + (w & 3) * 4 + (u == 52) * 512) = csB[u == 52][0];
      waitl<0>();
      if constexpr (u == U_F1) STAMPB(6);
      if constexpr (u == 4) waitv<nops(27) - 1 - rankI(48) + bsum(0, u)>();
      if constexpr (u == 12) waitc<bsum(0, u) + 2, bsum(0, u), B_U12>(fl_);
      if constexpr (u == U_F1) waitv<bsum(8, u)>();
      if constexpr (u == U_FE) waitv<bsum(20, u)>();
      if constexpr (u == 36 || u == 52) waitv<bsum(bslot(2 * breg(u) + 1) + 1, u)>();
      if constexpr (u == U_ATT) { STAMPB(8); poll<bsum(U_POLL + 1, U_ATT), B_7>(fl, fo1, rs, tag, fl_); }
      TIE(accB[t]); TIE(csB[t]);
      BAR();
      if constexpr (u == 12) STAMPB(5);
      if constexpr (u == U_ATT) STAMPB(9);
      if constexpr (u == U_F1) STAMPB(7);
      if constexpr (u == U_F1 || u == U_FE) raise<B_6>(fme + (u == U_F1 ? 0 : WS_FE - WS_F1), rs, tagv, fl_);
      if constexpr (u == 28 || u == 52) cp[0] = *(const LAS f32x4*)(uintptr_t)(aC + (u == 52) * 512);
    }
    if constexpr ((u & 1) == 0) PRIO(3 - (((u + 4) >> 1) & 3));
    if constexpr (breg(u) >= 0) { constexpr int R = breg(u); rdW2(IC<R>{}, w2[R]); SCHED(); }
    if constexpr (u + 4 < 56) rdB(IC<u + 4>{});
    if constexpr (u < 4) {
      hd[2 * (u & 1)] = pk(acc[0][4 * u] * rstd, acc[0][4 * u + 1] * rstd);
      hd[2 * (u & 1) + 1] = pk(acc[0][4 * u + 2] * rstd, acc[0][4 * u + 3] * rstd);
      if constexpr (u & 1) __builtin_amdgcn_raw_buffer_store_b128(v4u{hd[0], hd[1], hd[2], hd[3]}, rs, oQ + (u >> 1) * 1024, 0, AUX_SC1);
      else { TIE(hd[0]); TIE(hd[1]); }
    } else if constexpr (u < 20) {
      constexpr int e = u - 4;
      if constexpr (e < 4) {
        if (!typeA) {
          constexpr int T = 1 + (e >> 1), f = 8 * (e & 1);
          v4u o;
#pragma unroll
          for (int d = 0; d < 4; ++d) o[d] = pk(acc[T][f + 2 * d] * rstd, acc[T][f + 2 * d + 1] * rstd);
          __builtin_amdgcn_raw_buffer_store_b128(o, rs, oQ + T * 4096 + (e & 1) * 1024, 0, AUX_SC1);
        }
      }
      const float hval = silu_k(acc[1][e], k1s, ik2) * acc[2][e];
      if constexpr (e & 1) { hd[(e >> 1) & 3] = pk(hprev, hval); TIE(hd[(e >> 1) & 3]); }
      else { hprev = hval; TIE(hprev); }
      if constexpr ((e & 7) == 7) __builtin_amdgcn_raw_buffer_store_b128(v4u{hd[0], hd[1], hd[2], hd[3]}, rs, vl + TV(T_E) + (e >> 3) * 1024, 0, AUX_SC1);
    }
    if constexpr (u == 29 || u == 53) { cf[0] = csfrag(cp[0]); TIE(cf[0]); }
    if constexpr (u >= 32 && u < 52 && (u - 32) % 5 != 4) {
      constexpr int e = (u - 32) - (u - 32) / 5;
      sg[e] = silu_k(accB[0][e], k1s, ik2);
      TIE(sg[e]);
    }
    if constexpr (u == U_ATT) { ld16s(qf, la, rs, TV(T_AQ)); ld16s(k0, la, rs, TV(T_K0)); }
    if constexpr (u == U_ATT + 1) { ld16s(k1, la, rs, TV(T_K1)); ld16s(v0, la, rs, TV(T_V0)); }
    if constexpr (u == U_ATT + 2) ld16s(v1, la, rs, TV(T_V1));
    if constexpr (bw2(u) >= 0) issueWh(IC<(bw2(u) >> 1)>{}, IC<(bw2(u) & 1)>{});
    if constexpr (u == U_POLL) ld8c<B_7>(fl, fo1, rs, fl_);
    if constexpr (u == 55) ld8c<B_6>(fl, fo1, rs, fl_);
    SCHED();
    if constexpr (u == 0) csB[0] = mm16(sel, wb[0], csB[0]);
    if constexpr (u + 2 < 56 && ((u + 2) & 3) == 0) csB[u + 2 >= 28] = mm16(sel, wb[(u + 2) % 6], csB[u + 2 >= 28]);
    if constexpr (u == 30 || u == 54) accB[u == 54] = mm32(cf[0], mu, accB[u == 54]);
  });
  SCHED();
  PRIO(3);
  STAMP(4); CSTAMP(14);
#pragma unroll
  for (int c = 0; c < 2; ++c) {
#pragma unroll
    for (int e = 0; e < 4; ++e) hv[e] = pk(sg[8 * c + 2 * e] * accB[1][8 * c + 2 * e], sg[8 * c + 2 * e + 1] * accB[1][8 * c + 2 * e + 1]);
    __builtin_amdgcn_raw_buffer_store_b128(hv, rs, vl + TV(T_H) + 24 * 2048 + c * 1024, 0, AUX_SC1);
  }
  SCHED();
  STAMPN(5);
  unsigned lane_ = __builtin_amdgcn_mbcnt_hi(~0u, __builtin_amdgcn_mbcnt_lo(~0u, 0u));
  TIE(lane_);
  const unsigned n16_ = lane_ & 15, g4_ = lane_ >> 4, q4_ = n16_ >> 2, p4_ = lane_ & 3, tok_ = lane_ & 31, hh_ = lane_ >> 5, l7_ = lane_ & 7;
  const unsigned vA = vl + TV(T_A), vB = vl + TV(T_B);
  unsigned sT = HO_T;
  TIES(sT);
  v4u af[19][2];
  auto ldE2 = [&](auto i_) AI {
    constexpr int i = i_;
    ld16x4c<(i & 3) * 1024, B_H>(af[i][0], af[i][1], af[i + 1][0], af[i + 1][1], vA + (i >> 2) * 4096, rs, sT, fl_);
  };
  auto ldL = [&](auto i_) AI {
    constexpr int i = i_;
    if constexpr (i == 18) ld16x2c<(i & 3) * 1024, B_18>(af[i][0], af[i][1], vB + ((i - 8) >> 2) * 4096, rs, sT, fl_);
    else ld16x2<(i & 3) * 1024>(af[i][0], af[i][1], vB + ((i - 8) >> 2) * 4096, rs, sT);
  };
  waitv<bsum(U_ATT + 3, 56) + 2>();
  TIE(qf); TIE(k0); TIE(k1); TIE(v0); TIE(v1);
  {
    if (odd && g4_ == 0) { qf[0] = 0; qf[1] = 0; }
    if (!odd && g4_ == 3) { qf[2] = 0; qf[3] = 0; }
    constexpr unsigned U4 = wunit(4);
    const unsigned scr = U4 + w * 2048;
    *(LAS v4u*)(uintptr_t)(scr + n16_ * 64 + g4_ * 16) = v0;
    *(LAS v4u*)(uintptr_t)(scr + 1024 + n16_ * 64 + g4_ * 16) = v1;
    const f32x4 z4 = 0.f;
    const f32x4 sc0 = mm16(asb(k0), asb(qf), z4), sc1 = mm16(asb(k1), asb(qf), z4);
    bf16x8 vt[2];
#pragma unroll
    for (int t = 0; t < 2; ++t) {
      const unsigned a = scr + (4 * g4_ + q4_) * 64 + (((2 * p4_ + t + odd) * 8) & 63);
      const v4s lo = __builtin_amdgcn_ds_read_tr16_b64_v4i16((LAS v4s*)(uintptr_t)a);
      const v4s hi = __builtin_amdgcn_ds_read_tr16_b64_v4i16((LAS v4s*)(uintptr_t)(a + 1024));
      vt[t] = __builtin_shufflevector(lo, hi, 0, 1, 2, 3, 4, 5, 6, 7);
    }
    const bool par = n16_ & 1, c0 = n16_ >= 4 * g4_, c1 = n16_ >= 4 * g4_ + 2, back = (p | w) != 0;
    const float ninf = -3.0e38f, sc = 0.1889822365046136f * 1.4426950408889634f;
    const float x0 = par ? sc0[1] : sc0[0], x1 = par ? sc0[3] : sc0[2], y0 = par ? sc1[1] : sc1[0], y1 = par ? sc1[3] : sc1[2];
    float s0 = (c0 ? y0 : x0) * sc, s1 = (c1 ? y1 : x1) * sc;
    s0 = (c0 || back) ? s0 : ninf; s1 = (c1 || back) ? s1 : ninf;
    const float mx = rows4<true>(__builtin_fmaxf(s0, s1));
    const float e0 = __builtin_amdgcn_exp2f(s0 - mx), e1 = __builtin_amdgcn_exp2f(s1 - mx);
    const float inv = __builtin_amdgcn_rcpf(rows4<false>(e0 + e1));
    const unsigned pe = pk(e0 * inv, e1 * inv);
    const unsigned u0 = par ? pe << 16 : pe & 0xffffu, u1 = par ? pe & 0xffff0000u : pe >> 16;
    const v4u pf = {c0 ? 0u : u0, c1 ? 0u : u1, c0 ? u0 : 0u, c1 ? u1 : 0u};
    const f32x4 oa = mm16(vt[0], asb(pf), z4), ob = mm16(vt[1], asb(pf), z4);
    v4u ov = {pk(oa[0], oa[1]), pk(oa[2], oa[3]), pk(ob[0], ob[1]), pk(ob[2], ob[3])};
    if (g4_ == 3) { ov[2] = 0; ov[3] = 0; }
    __builtin_amdgcn_raw_buffer_store_b128(ov, rs, la + TV(T_O), 0, AUX_SC1);
  }
  SCHED();
  STAMPN(6);
  waitv<3>();
  sfor<4>([&](auto i) AI { rdW2(IC<5 + i>{}, w2[5 + i]); });
  poll<3, B_6>(fl, fo1, rs, tag, fl_);
  if (w == 6) *(LAS unsigned*)(uintptr_t)(L_CNT + 12) = 1u;
  waitw(L_CNT + 12, 1u);
  sfor<NPRE>([&](auto i) AI { ldE2(IC<2 * i>{}); });
  waitc<4 * NPRE, 0, B_H>(fl_);
  arrive(L_CNT, fme + (WS_F2 - WS_F1), rs, tagv);
  STAMPN(7);
  if (!owave) PRIO(0);
  if (!p2) {
    if (b == 255 && w == 0) {
#pragma unroll 1
      for (int k = 0; k < 4; ++k) poll<0, 0>(~tag, WS_F1 + (k * 4 + g4_) * FSTR + n16_ * 8, rs, tag, 1u);
      raise<0>(0u, rs, tagv, 1u);
    }
    return;
  }

  {
    const unsigned fo2 = n16_ * 8 + TV(T_F2);
    const unsigned vo = (lane_ >> 3) * 896 + l7_ * 16 + TV(T_OUT);
    v4u xres;
    f32x16 c2[2][2];
    bf16x8 wl[2][2];
#pragma unroll
    for (int t = 0; t < 4; ++t) c2[t >> 1][t & 1] = 0.f;
    auto step = [&](auto i_) AI {
      constexpr int i = i_;
      const bf16x8* wp = i < 9 ? w2[i < 9 ? i : 0] : wl[i & 1];
      if constexpr (i >= 8 && i < 18) { rdW2(IC<i + 1>{}, wl[(i + 1) & 1]); SCHED(); }
      TIE(af[i][0]); TIE(af[i][1]);
      c2[0][0] = mm32(wp[0], asb(af[i][0]), c2[0][0]);
      c2[0][1] = mm32(wp[0], asb(af[i][1]), c2[0][1]);
      c2[1][0] = mm32(wp[1], asb(af[i][0]), c2[1][0]);
      c2[1][1] = mm32(wp[1], asb(af[i][1]), c2[1][1]);
      SCHED();
    };
    sfor<4 - NPRE>([&](auto i) AI { ldE2(IC<2 * (NPRE + i)>{}); });
    sfor<18 - WREST>([&](auto i) AI { issueW(IC<WREST + i>{}); });
    asm volatile("global_load_dwordx4 %0, %1, %2" : "=v"(xres) : "v"(vo), "s"(x) : "memory");
    if (!owave) { waitv<10 + 2 * (18 - WREST) + 1>(); step(IC<0>{}); step(IC<1>{}); step(IC<2>{}); }
    if (owave) watch(fo2, rs, tag, L_CNT + 8, TV(T_NEED)); else waitw(L_CNT + 8, TV(T_NEED));
    PRIO(0);
    STAMPN(8);
    ld16x10r(af[3], af[4], af[5], af[6], af[7], vA, vA + 4096, rs, sT, fl_);
    sfor<5>([&](auto i) AI { ldL(IC<8 + i>{}); });
    sfor<5>([&](auto i) AI {
      waitv<18>();
      step(IC<3 + i>{});
      ldL(IC<13 + i>{});
    });
    ldL(IC<18>{});
    sfor<10>([&](auto i) AI { waitv<2 * (9 - i)>(); step(IC<8 + i>{}); });
    if (w < 4 || owave) step(IC<18>{});
    STAMPN(9);
#pragma unroll
    for (int nt = 0; nt < 2; ++nt)
#pragma unroll
      for (int tt = 0; tt < 2; ++tt)
#pragma unroll
        for (int c = 0; c < 2; ++c) {
          v4u o;
#pragma unroll
          for (int e = 0; e < 4; ++e) o[e] = pk(c2[nt][tt][8 * c + 2 * e], c2[nt][tt][8 * c + 2 * e + 1]);
          *(LAS v4u*)(uintptr_t)((2 * nt + c) * UNIT + w * 2048 + hh_ * 1024 + (((32 * tt + tok_) ^ (4 * (2 * c + hh_))) * 16)) = o;
        }
    WAITL0();
    BAR();
    STAMP(10);
    float f[8], sv[8];
    const unsigned ar = (l7_ >> 1) * UNIT + (l7_ & 1) * 1024 + (((8 * w + (lane_ >> 3)) ^ (4 * (l7_ & 3))) * 16);
#pragma unroll
    for (int k = 0; k < 8; ++k) {
      const v4u v = *(const LAS v4u*)(uintptr_t)(ar + k * 2048);
#pragma unroll
      for (int e = 0; e < 4; ++e) {
        if (k == 0) { f[2 * e] = blo(v[e]); f[2 * e + 1] = bhi(v[e]); }
        else if (k < 6) { f[2 * e] += blo(v[e]); f[2 * e + 1] += bhi(v[e]); }
        else if (k == 6) { sv[2 * e] = blo(v[e]); sv[2 * e + 1] = bhi(v[e]); }
        else { sv[2 * e] += blo(v[e]); sv[2 * e + 1] += bhi(v[e]); }
      }
    }
    TIE(xres);
    float r[8];
#pragma unroll
    for (int e = 0; e < 8; ++e) {
      const float g = f[e] * __builtin_amdgcn_rcpf(1.0f + __builtin_amdgcn_exp2f(f[e] * -1.4426950408889634f));
      r[e] = __builtin_fmaf(sv[e], g, (e & 1) ? bhi(xres[e >> 1]) : blo(xres[e >> 1]));
    }
    const v4u o = {pk(r[0], r[1]), pk(r[2], r[3]), pk(r[4], r[5]), pk(r[6], r[7])};
    *(GAS v4u*)(out + vo) = o;
    STAMP(11); CSTAMP(15);
  }
}

extern "C" void kernel_launch(void* const* d_in, const int* in_sizes, int n_in, void* d_out, int out_size, void* d_ws, size_t ws_size,
                              hipStream_t stream) {
  (void)in_sizes; (void)n_in; (void)out_size; (void)ws_size;
  (void)hipFuncSetAttribute((const void*)fused_block, hipFuncAttributeMaxDynamicSharedMemorySize, 163840);
  fused_block<<<256, 512, 163840, stream>>>((const char*)d_in[0], (const char*)d_in[1], (const char*)d_in[2], (const char*)d_in[4],
                                            (const char*)d_in[5], (const char*)d_in[6], (char*)d_out, (char*)d_ws);
}
```

```cpp
#include <hip/hip_runtime.h>
#include <stdint.h>
#include <utility>

#define GAS __attribute__((address_space(1)))
#define LAS __attribute__((address_space(3)))
#define AI __attribute__((always_inline))
typedef unsigned v4u __attribute__((ext_vector_type(4)));
typedef short v4s __attribute__((ext_vector_type(4)));
typedef short bf16x8 __attribute__((ext_vector_type(8)));
typedef float f32x4 __attribute__((ext_vector_type(4)));
typedef float f32x16 __attribute__((ext_vector_type(16)));
typedef float v2f __attribute__((ext_vector_type(2)));
typedef __bf16 v2bf __attribute__((ext_vector_type(2)));
typedef unsigned long long u64;

namespace {
constexpr unsigned FSTR = 4224;
constexpr unsigned WS_F1 = 65536, WS_FE = WS_F1 + 16 * FSTR, WS_F2 = WS_FE + 16 * FSTR;
constexpr unsigned WS_ST = 32768;
constexpr unsigned WS_QKV = 393216, QKV_T = 168 * 512;
constexpr unsigned WS_HO = WS_QKV + 64 * QKV_T, HO_T = 288 * 512;
constexpr unsigned WS_END = WS_HO + 64 * HO_T;
constexpr unsigned WS_DBG = 32u << 20;
constexpr u64 TAG_STEP = 0x9E3779B97F4A7C15ull;
constexpr int AUX_SC1 = 16;
constexpr unsigned SLOT = 40960, UNIT = 16384;
constexpr unsigned L_CSA = 122880, L_CNT = L_CSA + 3072;
constexpr unsigned CODE_BYTES = 19968;
constexpr unsigned OOB = 0x40000000u;
constexpr unsigned bunit(int s) { return (s == 0 ? 8 : s == 1 ? 9 : s == 2 ? 0 : s == 3 ? 1 : s == 4 ? 3 : s == 5 ? 4 : 5) * UNIT; }
constexpr unsigned wunit(int i) {
  return i == 18 ? 7 * UNIT : (i % 9) == 0 ? 2 * UNIT : (i % 9) == 1 ? 6 * UNIT : bunit(i % 9 - 2);
}
#define NPRE 1
constexpr int PRO = 10, G_ST = 17;
constexpr int avail(int g) {
  return g < 2 ? 20 : g < 6 ? 25 : g < 10 ? 30 : g < 14 ? 35 : g < 18 ? 39 : g < 22 ? 43 : g < 26 ? 49 : 53;
}
constexpr int cum(int g) {
  int c = PRO;
  for (int k = 0; k <= g; ++k) {
    int n = c + ((k & 1) && k != 27 ? 1 : 2);
    c = n < avail(k) ? n : avail(k);
  }
  return c;
}
static_assert(cum(27) == 53, "every item issued");
constexpr int nextra(int g) { return (g == 1) + (g == 7) + (g == G_ST); }
constexpr int nops(int g) {
  int c = PRO + 3;
  for (int k = 0; k <= g; ++k) c += cum(k) - cum(k - 1) + nextra(k);
  return c;
}
constexpr int rankI(int it) {
  if (it < PRO) return it < 5 ? it + 1 : it + 3;
  int g = 0;
  while (cum(g) <= it) ++g;
  return nops(g - 1) + it - cum(g - 1);
}
__device__ __forceinline__ unsigned umin(unsigned a, unsigned b) { return a < b ? a : b; }
constexpr int imin(int a, int b) { return a < b ? a : b; }
constexpr int U_F1 = 28, U_FE = 44, U_POLL = 38, U_ATT = 44;
#define PLAN 2
constexpr int bw2(int u) {
  return u == 8 ? 4 : u == 9 ? 5 : u == 10 ? 36 : u == 14 ? 6 : u == 16 ? 7 : u == 29 ? 18 : u == 30 ? 19 : u == 32 ? 8 : u == 33 ? 9
       : u == 35 ? 10 : u == 36 ? 11 : u == 39 ? 12 : u == 40 ? 13 : u == 42 ? 20 : u == 43 ? 21 : u == 48 ? 14 : u == 50 ? 15
       : u == 54 ? 16 : u == 55 ? 17 : -1;
}
constexpr int WREST = PLAN == 2 ? 11 : 13;
constexpr int breg(int u) { return u == 5 ? 0 : u == 13 ? 1 : u == 36 ? 2 : u == 44 ? 3 : u == 52 ? 4 : -1; }
constexpr int bops(int u) {
  const int p = bw2(u) >> 1;
  return (u == 1 || u == 3 || u == 11 || u == 19) + (bw2(u) >= 0 && p != 18) + (u == U_ATT || u == U_ATT + 1 ? 2 : u == U_ATT + 2 ? 1 : 0);
}
constexpr int bsum(int u0, int u1) { int c = 0; for (int u = u0; u < u1; ++u) c += bops(u); return c; }
constexpr int bslot(int piece) { int u = 0; while (bw2(u) != piece) ++u; return u; }

enum { T_Q, T_H, T_E, T_S, T_AQ, T_K0, T_K1, T_V0, T_V1, T_DA, T_DB, T_D18, T_F0, T_F1, T_ME, T_FLAGS,
       T_A, T_B, T_OUT, T_F2, T_NEED, T_O, T_STP, T_CS, T_WM, T_WMN, T_WMX, T_W2N, T_AWB, T_N = 32 };
enum { B_O, B_H, B_18, B_U12, B_7, B_6 };
struct Tab {
  unsigned v[2048][T_N];
  constexpr Tab() : v() {
    for (unsigned b = 0; b < 256; ++b)
      for (unsigned w = 0; w < 8; ++w) {
        unsigned* t = v[b * 8 + w];
        const unsigned xl = b & 7, idx = b >> 3, rg = w & 3, set = w >> 2;
        const bool typeA = (idx & 3) < 3, p2 = idx < 28, ow = w >= 6;
        const unsigned j = typeA ? 3 * (xl & 3) + (idx & 3) : 12 + (xl & 3), p = 8 * (xl >> 2) + (idx >> 2);
        const unsigned qb0 = typeA ? j : j == 15 ? 18 : 3 * j - 24, tt1 = 4 * p + rg;
        const unsigned rg2 = p2 ? 4 * xl + idx / 7 : 2 * p, cg = idx % 7, kL = w < 5 ? 11 * w : 54;
        t[T_Q] = j < 15 ? WS_QKV + tt1 * QKV_T + (2 * qb0 + set) * 2048 : OOB;
        t[T_H] = WS_HO + tt1 * HO_T + (2 * j + set) * 2048;
        t[T_E] = typeA ? t[T_H] : OOB;
        t[T_S] = WS_ST + tt1 * 512;
        const unsigned aq = WS_QKV + (4 * p + (w >> 1)) * QKV_T + (w & 1) * 256 + ((7 * j) >> 1) * 512;
        const unsigned ak = aq - ((w & 1) ? 256u : (p | w) ? QKV_T - 256u : 0u);
        t[T_AQ] = aq; t[T_K0] = ak + 56 * 512; t[T_K1] = aq + 56 * 512; t[T_V0] = ak + 112 * 512; t[T_V1] = aq + 112 * 512;
        t[T_O] = WS_HO + (4 * p + (w >> 1)) * HO_T + (w & 1) * 256 + (224 + 4 * j) * 512;
        t[T_DA] = p2 ? cg * 128 + (ow ? (w - 6) * 224 + 112 : 128 * w) * 896 : OOB;
        t[T_DB] = p2 ? cg * 128 + (ow ? (w - 6) * 224 : 768 + 16 * kL) * 896 : OOB;
        t[T_D18] = ow ? 140 * 896 : 160 * 896;
        t[T_STP] = ow ? 4 * 896 + 1024 : 8 * 896 + 1024;
        t[T_W2N] = ow ? 448 * 896 : 1792 * 896;
        t[T_A] = WS_HO + 2 * rg2 * HO_T + (ow ? 120 + 16 * (w - 6) : 8 * w) * 1024;
        t[T_B] = WS_HO + 2 * rg2 * HO_T + (ow ? 112 + 16 * (w - 6) : 48 + kL) * 1024;
        t[T_OUT] = (64 * rg2 + 8 * w) * 896 + cg * 128;
        t[T_F0] = w == 6 ? WS_FE + (rg2 >> 1) * FSTR : WS_F1 + p * FSTR;
        t[T_F1] = w != 6 && p > 0 ? FSTR : 0;
        t[T_F2] = WS_F2 + (rg2 >> 1) * FSTR;
        t[T_ME] = WS_F1 + p * FSTR + j * 8;
        t[T_NEED] = ow ? 0xffu << (8 * (w - 6)) : ((2u << ((kL + (w < 4 ? 10 : 9)) >> 2)) - 1) & ~((1u << (kL >> 2)) - 1);
        const bool c18 = w < 4 || ow;
        t[T_FLAGS] = ow << B_O | (p2 && !ow) << B_H | c18 << B_18 | (c18 || !typeA) << B_U12 | (w == 7) << B_7 | (w == 6) << B_6;
        t[T_CS] = L_CSA + set * 1536;
        t[T_AWB] = 8 * UNIT - 2 * (rg ^ (2 * set)) * 1024;
        const unsigned n = w < 4 ? 14 : w < 7 ? 17 : 13;
        t[T_WM] = (w < 4 ? b * 2 + (w & 1) : w < 7 ? b * 3 + w - 4 : b) * n; t[T_WMN] = n - 1; t[T_WMX] = w < 7 ? 12543u : 3135u;
      }
  }
};
__constant__ const Tab kTab = Tab();
typedef unsigned v16u __attribute__((ext_vector_type(16)));

template <int N> using IC = std::integral_constant<int, N>;
template <int... I, class F> __device__ __forceinline__ void sfor_(std::integer_sequence<int, I...>, F&& f) { (f(std::integral_constant<int, I>{}), ...); }
template <int N, class F> __device__ __forceinline__ void sfor(F&& f) { sfor_(std::make_integer_sequence<int, N>{}, f); }

#define WAITL0() asm volatile("s_waitcnt lgkmcnt(0)" ::: "memory")
#define BAR() do { asm volatile("" ::: "memory"); __builtin_amdgcn_s_barrier(); asm volatile("" ::: "memory"); } while (0)
#define SCHED() __builtin_amdgcn_sched_barrier(0)
#define PRIO(n) __builtin_amdgcn_s_setprio(n)
template <class T> __device__ __forceinline__ void TIE(T& v) { asm volatile("" : "+v"(v)); }
template <class T> __device__ __forceinline__ void TIES(T& v) { asm volatile("" : "+s"(v)); }
template <int N> __device__ __forceinline__ void waitv() { asm volatile("s_waitcnt vmcnt(%0)" ::"i"(N) : "memory"); }
template <int N> __device__ __forceinline__ void waitl() { __builtin_amdgcn_s_waitcnt(0xC07F | (N << 8)); }

typedef __amdgpu_buffer_rsrc_t rsrc_t;
__device__ __forceinline__ rsrc_t mkrs(const char* p, unsigned bytes) {
  rsrc_t r = __builtin_amdgcn_make_buffer_rsrc((void*)p, 0, bytes, 0x00020000);
  TIES(r);
  return r;
}
template <unsigned L> __device__ __forceinline__ void setm0(unsigned ldsw) {
  asm volatile("s_add_u32 m0, %0, %1\n\ts_nop 0" ::"s"(ldsw), "i"(L) : "memory", "scc");
}
template <unsigned O> __device__ __forceinline__ void dmag(unsigned voff, const char* base) {
  asm volatile("global_load_lds_dwordx4 %0, %1 offset:%2" ::"v"(voff), "s"(base), "i"(O) : "memory");
}
template <unsigned O> __device__ __forceinline__ void dmab(unsigned voff, rsrc_t rs) {
  asm volatile("buffer_load_dwordx4 %0, %1, 0 offen offset:%2 lds" ::"v"(voff), "s"(rs), "i"(O) : "memory");
}
template <unsigned L, int B> __device__ __forceinline__ void dmab2c(unsigned v0, unsigned v1, rsrc_t rs, unsigned ldsw, unsigned c) {
  asm volatile("s_bitcmp0_b32 %5, %6\n\ts_cbranch_scc1 1f\n\ts_add_u32 m0, %3, %4\n\ts_nop 0\n\tbuffer_load_dwordx4 %0, %2, 0 offen lds\n\t"
               "buffer_load_dwordx4 %1, %2, 0 offen offset:1024 lds\n1:" ::"v"(v0), "v"(v1), "s"(rs), "s"(ldsw), "i"(L), "s"(c), "i"(B) : "memory", "scc");
}
__device__ __forceinline__ void ld16(v4u& d, unsigned voff, rsrc_t rs) {
  asm volatile("buffer_load_dwordx4 %0, %1, %2, 0 offen sc1" : "=v"(d) : "v"(voff), "s"(rs) : "memory");
}
__device__ __forceinline__ void ld16s(v4u& d, unsigned voff, rsrc_t rs, unsigned so) {
  asm volatile("buffer_load_dwordx4 %0, %1, %2, %3 offen sc1" : "=v"(d) : "v"(voff), "s"(rs), "s"(so) : "memory");
}
template <unsigned O> __device__ __forceinline__ void ld16x2(v4u& d0, v4u& d1, unsigned v, rsrc_t rs, unsigned so) {
  asm volatile("buffer_load_dwordx4 %0, %2, %3, 0 offen offset:%5 sc1\n\tbuffer_load_dwordx4 %1, %2, %3, %4 offen offset:%5 sc1"
               : "=&v"(d0), "=&v"(d1) : "v"(v), "s"(rs), "s"(so), "i"(O) : "memory");
}
template <unsigned O, int B> __device__ __forceinline__ void ld16x2c(v4u& d0, v4u& d1, unsigned v, rsrc_t rs, unsigned so, unsigned c) {
  asm volatile("s_bitcmp0_b32 %6, %7\n\ts_cbranch_scc1 1f\n\tbuffer_load_dwordx4 %0, %2, %3, 0 offen offset:%5 sc1\n\t"
               "buffer_load_dwordx4 %1, %2, %3, %4 offen offset:%5 sc1\n1:"
               : "=&v"(d0), "=&v"(d1) : "v"(v), "s"(rs), "s"(so), "i"(O), "s"(c), "i"(B) : "memory", "scc");
}
template <unsigned O, int B> __device__ __forceinline__ void ld16x4c(v4u& d0, v4u& d1, v4u& d2, v4u& d3, unsigned v, rsrc_t rs, unsigned so, unsigned c) {
  asm volatile("s_bitcmp0_b32 %9, %10\n\ts_cbranch_scc1 1f\n\tbuffer_load_dwordx4 %0, %4, %5, 0 offen offset:%7 sc1\n\t"
               "buffer_load_dwordx4 %1, %4, %5, %6 offen offset:%7 sc1\n\tbuffer_load_dwordx4 %2, %4, %5, 0 offen offset:%8 sc1\n\t"
               "buffer_load_dwordx4 %3, %4, %5, %6 offen offset:%8 sc1\n1:"
               : "=&v"(d0), "=&v"(d1), "=&v"(d2), "=&v"(d3) : "v"(v), "s"(rs), "s"(so), "i"(O), "i"(O + 1024), "s"(c), "i"(B) : "memory", "scc");
}
__device__ __forceinline__ void ld16x10r(v4u* a, v4u* b, v4u* c_, v4u* d, v4u* e, unsigned v0, unsigned v1, rsrc_t rs, unsigned so, unsigned c) {
  asm volatile("s_bitcmp0_b32 %14, 0\n\ts_cbranch_scc1 1f\n\t"
               "buffer_load_dwordx4 %0, %10, %12, 0 offen offset:3072 sc1\n\tbuffer_load_dwordx4 %1, %10, %12, %13 offen offset:3072 sc1\n\t"
               "buffer_load_dwordx4 %2, %11, %12, 0 offen sc1\n\tbuffer_load_dwordx4 %3, %11, %12, %13 offen sc1\n\t"
               "buffer_load_dwordx4 %4, %11, %12, 0 offen offset:1024 sc1\n\tbuffer_load_dwordx4 %5, %11, %12, %13 offen offset:1024 sc1\n\t"
               "buffer_load_dwordx4 %6, %11, %12, 0 offen offset:2048 sc1\n\tbuffer_load_dwordx4 %7, %11, %12, %13 offen offset:2048 sc1\n\t"
               "buffer_load_dwordx4 %8, %11, %12, 0 offen offset:3072 sc1\n\tbuffer_load_dwordx4 %9, %11, %12, %13 offen offset:3072 sc1\n1:"
               : "+v"(a[0]), "+v"(a[1]), "+v"(b[0]), "+v"(b[1]), "+v"(c_[0]), "+v"(c_[1]), "+v"(d[0]), "+v"(d[1]), "+v"(e[0]), "+v"(e[1])
               : "v"(v0), "v"(v1), "s"(rs), "s"(so), "s"(c) : "memory", "scc");
}
__device__ __forceinline__ void touch(unsigned& d, unsigned voff, const char* base) {
  asm volatile("global_load_dword %0, %1, %2" : "=v"(d) : "v"(voff), "s"(base) : "memory");
}
template <int N1, int N0, int B> __device__ __forceinline__ void waitc(unsigned c) {
  static_assert(N0 <= N1 && N0 >= 0, "");
  asm volatile("s_bitcmp1_b32 %0, %3\n\ts_cbranch_scc1 1f\n\ts_waitcnt vmcnt(%2)\n1:\n\ts_waitcnt vmcnt(%1)" ::"s"(c), "i"(N1), "i"(N0), "i"(B) : "memory", "scc");
}
__device__ __forceinline__ float wave_sum63(float v) {
#define DPP_ADD(ctrl, rm) v += __builtin_bit_cast(float, __builtin_amdgcn_update_dpp(0, __builtin_bit_cast(int, v), ctrl, rm, 0xf, true));
  DPP_ADD(0x111, 0xf) DPP_ADD(0x112, 0xf) DPP_ADD(0x114, 0xf) DPP_ADD(0x118, 0xf) DPP_ADD(0x142, 0xa) DPP_ADD(0x143, 0xc)
#undef DPP_ADD
  return v;
}
__device__ __forceinline__ bf16x8 ldtr(unsigned a) {
  v4s lo = __builtin_amdgcn_ds_read_tr16_b64_v4i16((LAS v4s*)(uintptr_t)a);
  v4s hi = __builtin_amdgcn_ds_read_tr16_b64_v4i16((LAS v4s*)(uintptr_t)(a + 512));
  return __builtin_shufflevector(lo, hi, 0, 1, 2, 3, 4, 5, 6, 7);
}
__device__ __forceinline__ unsigned pk(float a, float b) {
  v2f f = {a, b};
  return __builtin_bit_cast(unsigned, __builtin_convertvector(f, v2bf));
}
__device__ __forceinline__ float asf(unsigned d) { return __builtin_bit_cast(float, d); }
__device__ __forceinline__ unsigned asu(float f) { return __builtin_bit_cast(unsigned, f); }
__device__ __forceinline__ float blo(unsigned d) { return __builtin_bit_cast(float, d << 16); }
__device__ __forceinline__ float bhi(unsigned d) { return __builtin_bit_cast(float, d & 0xffff0000u); }
__device__ __forceinline__ f32x16 mm32(bf16x8 a, bf16x8 b, f32x16 c) { return __builtin_amdgcn_mfma_f32_32x32x16_bf16(a, b, c, 0, 0, 0); }
__device__ __forceinline__ f32x4 mm16(bf16x8 a, bf16x8 b, f32x4 c) { return __builtin_amdgcn_mfma_f32_16x16x32_bf16(a, b, c, 0, 0, 0); }
__device__ __forceinline__ bf16x8 asb(v4u v) { return __builtin_bit_cast(bf16x8, v); }
template <bool MAX> __device__ __forceinline__ float rows4(float v) {
  {
    const auto r = __builtin_amdgcn_permlane16_swap(asu(v), asu(v), false, false);
    const unsigned a = r[0], b = r[1];
    v = MAX ? __builtin_fmaxf(asf(a), asf(b)) : asf(a) + asf(b);
  }
  const auto r = __builtin_amdgcn_permlane32_swap(asu(v), asu(v), false, false);
  const unsigned a = r[0], b = r[1];
  return MAX ? __builtin_fmaxf(asf(a), asf(b)) : asf(a) + asf(b);
}
template <int B> __device__ __forceinline__ void raise(unsigned foff, rsrc_t rs, u64 tv, unsigned c) {
  asm volatile("s_bitcmp0_b32 %3, %4\n\ts_cbranch_scc1 1f\n\ts_mov_b64 exec, 1\n\tbuffer_store_dwordx2 %0, off, %1, %2 sc1\n\ts_mov_b64 exec, -1\n1:"
               ::"v"(tv), "s"(rs), "s"(foff), "s"(c), "i"(B) : "memory", "scc");
}
__device__ __forceinline__ void arrive(unsigned cnt, unsigned foff, rsrc_t rs, u64 tv) {
  unsigned a = cnt, t;
  asm volatile("s_mov_b64 exec, 1\n\tv_mov_b32 %0, 1\n\tds_add_rtn_u32 %0, %1, %0\n\ts_waitcnt lgkmcnt(0)\n\t"
               "v_cmp_eq_u32 vcc, 7, %0\n\ts_cbranch_vccz 1f\n\tbuffer_store_dwordx2 %2, off, %3, %4 sc1\n1:\n\ts_mov_b64 exec, -1"
               : "=&v"(t) : "v"(a), "v"(tv), "s"(rs), "s"(foff) : "memory", "vcc");
}
template <int B> __device__ __forceinline__ void ld8c(u64& d, unsigned voff, rsrc_t rs, unsigned c) {
  asm volatile("s_bitcmp0_b32 %3, %4\n\ts_cbranch_scc1 1f\n\tbuffer_load_dwordx2 %0, %1, %2, 0 offen sc1\n1:" : "=&v"(d) : "v"(voff), "s"(rs), "s"(c), "i"(B) : "memory", "scc");
}
template <int N, int B> __device__ __forceinline__ void poll(u64 v, unsigned voff, rsrc_t rs, u64 tag, unsigned c) {
  unsigned n = 1u << 19;
  asm volatile("s_bitcmp0_b32 %5, %7\n\ts_cbranch_scc1 2f\n\ts_waitcnt vmcnt(%6)\n1:\n\tv_cmp_ne_u64 vcc, %4, %0\n\ts_cbranch_vccz 2f\n\ts_sleep 3\n\t"
               "buffer_load_dwordx2 %0, %2, %3, 0 offen sc1\n\ts_sub_u32 %1, %1, 1\n\ts_waitcnt vmcnt(0)\n\ts_cbranch_scc0 1b\n2:"
               : "+v"(v), "+s"(n) : "v"(voff), "s"(rs), "s"(tag), "s"(c), "i"(N), "i"(B) : "memory", "vcc", "scc");
}
__device__ __forceinline__ void watch(unsigned voff, rsrc_t rs, u64 tag, unsigned lds, unsigned need) {
  unsigned n = 1u << 19, a = lds, t, m;
  u64 v;
  asm volatile("1:\n\tbuffer_load_dwordx2 %0, %4, %5, 0 offen sc1\n\ts_waitcnt vmcnt(0)\n\tv_cmp_eq_u64 vcc, %6, %0\n\ts_mov_b32 %3, vcc_lo\n\t"
               "v_mov_b32 %2, %3\n\ts_mov_b64 exec, 1\n\tds_or_b32 %7, %2\n\ts_mov_b64 exec, -1\n\t"
               "s_andn2_b32 %3, %8, %3\n\ts_cbranch_scc0 2f\n\ts_sleep 1\n\ts_sub_u32 %1, %1, 1\n\ts_cbranch_scc0 1b\n2:"
               : "=&v"(v), "+s"(n), "=&v"(t), "=&s"(m) : "v"(voff), "s"(rs), "s"(tag), "v"(a), "s"(need) : "memory", "vcc", "scc");
}
__device__ __forceinline__ void waitw(unsigned lds, unsigned need) {
  unsigned a = lds, t, m, n = 1u << 22;
  asm volatile("1:\n\tds_read_b32 %0, %3\n\ts_waitcnt lgkmcnt(0)\n\tv_readfirstlane_b32 %2, %0\n\ts_andn2_b32 %2, %4, %2\n\ts_cbranch_scc0 2f\n\t"
               "s_sleep 1\n\ts_sub_u32 %1, %1, 1\n\ts_cbranch_scc0 1b\n2:"
               : "=&v"(t), "+s"(n), "=&s"(m) : "v"(a), "s"(need) : "memory", "scc");
}
__device__ __forceinline__ void pollg(v4u& v, unsigned voff, rsrc_t rs, u64 tag) {
  unsigned v1 = v[1], v3 = v[3];
  const unsigned lo = (unsigned)tag, hi = (unsigned)(tag >> 32);
  if (__builtin_amdgcn_ballot_w64(((v1 ^ lo) | (v3 ^ hi)) != 0) == 0) return;
#pragma unroll 1
  for (unsigned n = 1u << 20; n; --n) {
    v = __builtin_amdgcn_raw_buffer_load_b128(rs, voff, 0, AUX_SC1);
    v1 = v[1]; v3 = v[3];
    if (__builtin_amdgcn_ballot_w64(((v1 ^ lo) | (v3 ^ hi)) != 0) == 0) break;
    __builtin_amdgcn_s_sleep(1);
  }
}
__device__ __forceinline__ bf16x8 csfrag(f32x4 p) {
  const float c = (p[0] + p[1]) + (p[2] + p[3]);
  const unsigned h = pk(c, 0.f);
  const unsigned d = pk(c, c - blo(h));
  return asb(v4u{d, d, 0u, 0u});
}
__device__ __forceinline__ float silu_k(float g, float k1, float ik2) {
  return g * __builtin_amdgcn_rcpf(__builtin_fmaf(__builtin_amdgcn_exp2f(g * k1), ik2, ik2));
}
#define ENC 1
#define STAMP(i) do { if (i == 0 || i == ENC) { u64 t_; SCHED(); asm volatile("s_memrealtime %0\n\ts_waitcnt lgkmcnt(0)" : "=s"(t_)::"memory"); SCHED(); est[i == 0 ? 0 : 1] = t_; } } while (0)
#define CSTAMP(i)
#define STAMPB(i)
#define STAMPP(i)
#define STAMPN(i) STAMP(i)
}

#if defined(__HIP_DEVICE_COMPILE__)
__attribute__((target("no-packed-fp32-ops")))
#endif
__global__ __launch_bounds__(512, 2) void fused_block(const char* __restrict__ x, const char* __restrict__ wqkv, const char* __restrict__ wo,
                                                      const char* __restrict__ wg, const char* __restrict__ wu, const char* __restrict__ wd,
                                                      char* __restrict__ out, char* __restrict__ ws) {
  u64 est[2];
  STAMP(0);
  const unsigned tid = threadIdx.x, lane = tid & 63, wv = tid >> 6, b = blockIdx.x, xl = b & 7, idx = b >> 3;
  unsigned warm0, warm, warm2;
  {
    u64 pc;
    asm volatile("s_getpc_b64 %0" : "=s"(pc));
    touch(warm0, umin((((b & 56u) + wv) * 3 + (tid & 3)) * 128, CODE_BYTES - 128), (const char*)pc);
  }
  asm volatile("" ::"s"(x), "s"(wqkv), "s"(wo), "s"(wg), "s"(wu), "s"(wd), "s"(out), "s"(ws));
  const unsigned w = __builtin_amdgcn_readfirstlane(wv);
  v16u ta, tb;
  asm volatile("s_load_dwordx16 %0, %2, 0x0\n\ts_load_dwordx16 %1, %2, 0x40" : "=&s"(ta), "=&s"(tb) : "s"(kTab.v[b * 8 + w]) : "memory");
#define TV(i) ((i) < 16 ? ta[(i) & 15] : tb[(i) & 15])
  const bool typeA = (idx & 3) < 3;
  const unsigned j = typeA ? 3 * (xl & 3) + (idx & 3) : 12 + (xl & 3), p = 8 * (xl >> 2) + (idx >> 2);
  const unsigned lw2 = w * 2048, lw3 = w * 3072;

  STAMPP(5);
  const unsigned l3 = lane >> 3, l7 = lane & 7, r8 = tid >> 3;
  const unsigned cw = (l7 ^ ((lane >> 2) & 4)) * 16;
  const unsigned vx0 = (128 * p + 8 * wv + r8) * 896 + ((l7 ^ (lane >> 4)) * 16), vx1 = (vx0 ^ 64) + (8 * 896 - 1024);
  const unsigned qb0 = typeA ? j : umin(3 * j - 24, 18u);
  unsigned vq = r8 * 2688 + qb0 * 128 + cw, vf = r8 * 3584 + j * 128 + cw, v12 = typeA ? vf : vq;
  const char* b1 = (typeA ? wg : wqkv + 128) - 1024;
  const char* b2 = (typeA ? wu : wqkv + 256) - 2048;
  const unsigned st12 = typeA ? 64 * 3584 : 64 * 2688;
  auto itemA = [&](auto it_) AI {
    constexpr int it = it_, s = it / 5, k = it % 5;
    constexpr unsigned sl = (s & 3) * SLOT;
    if constexpr (k == 0) { setm0<sl + 24576 - s * 128>(lw2); dmag<s * 128>(vx0, x); }
    else if constexpr (k == 1) dmag<s * 128 + 1024>(vx1, x);
    else if constexpr (k == 2) { setm0<sl>(lw3); dmag<0>(vq, wqkv); vq += 64 * 2688; TIE(vq); }
    else if constexpr (k == 3) dmag<1024>(v12, b1);
    else { dmag<2048>(v12, b2); v12 += st12; TIE(v12); }
  };
  sfor<5>([&](auto it) AI { itemA(it); });
  BAR();
  const unsigned ts = 128 * p + 8 * j + wv;
  v4u xrow;
  asm volatile("global_load_dwordx4 %0, %1, %2" : "=v"(xrow) : "v"(ts * 896 + umin(lane, 55u) * 16), "s"(x) : "memory");
  u64 tag;
  asm volatile("s_load_dwordx2 %0, %1, 0x0" : "=s"(tag) : "s"(ws) : "memory");
  STAMPP(6);
  {
    const unsigned n = wv < 2 ? 19 : 7, k = wv < 2 ? wv : wv < 5 ? wv - 2 : wv - 5;
    const unsigned L = umin((b * (wv < 2 ? 2 : 3) + k) * n + umin(lane, n - 1), 9407u);
    touch(warm, L * 128 + (wv < 2 ? 0u : ((L * 43691u) >> 19) * 2048), w < 2 ? wqkv : w < 5 ? wg : wu);
  }
  SCHED();
  STAMPP(7);
  const unsigned rg = wv & 3, set = wv >> 2, rr = rg ^ (2 * set), tok = lane & 31, hh = lane >> 5;
  const unsigned q4 = (lane & 15) >> 2, p4 = lane & 3, sp4 = ((p4 & 1) << 1) | (p4 >> 1);
  const unsigned trl = q4 * 128 + (((lane >> 2) & 4) + sp4) * 8;
  const unsigned aw = trl + ((set ^ (q4 >> 1)) << 6);
  const unsigned ax = (32 * rg + tok) * 128 + (((2 * rr + hh) ^ ((tok >> 1) & 7)) << 4);
  const unsigned selw = (((lane >> 4) ^ (lane >> 2)) & 1) ? 0u : 0x3F803F80u;
  const bf16x8 sel = asb(v4u{selw, selw, selw, selw});
  unsigned bw[4], bx[4];
#pragma unroll
  for (int i = 0; i < 4; ++i) { bw[i] = aw + (2 * (rr ^ i) + hh) * 3072; bx[i] = (ax ^ (i * 32)) + 24576; TIE(bw[i]); TIE(bx[i]); }

  const rsrc_t rs = mkrs(ws, WS_END);
  unsigned vwm;
  const char* bwm = w < 2 ? wg : w < 4 ? wu : w < 7 ? wd : wo;
  const bool p2 = idx < 28, owave = w >= 6;
  const char* w2p = owave ? wo : wd;
  TIES(w2p);
  unsigned vdA, vdB, vdC, awB, a2, vl, oS, aC, la, fo1;
  rsrc_t rw2;
  auto setup = [&](auto n_) AI {
    constexpr int n = n_;
    unsigned l = lane;
    TIE(l);
    if constexpr (n == 4) {
      awB = bw[0] - (l >> 5) * 1024 + TV(T_AWB);
      a2 = ((bw[0] & 1023u) ^ ((w & 4) << 4)) + (l >> 5) * 1024 + lw2;
      TIE(awB); TIE(a2);
    } else if constexpr (n == 5) {
      oS = (l & 31) * 16 + TV(T_S); aC = (l & 31) * 16 + TV(T_CS); vl = l * 16;
      TIE(oS); TIE(aC); TIE(vl);
    } else if constexpr (n == 7) {
      la = (l & 15) * 16 + (l >> 4) * 512;
      TIE(la);
    } else if constexpr (n == 8) {
      vdA = (l >> 3) * 896 + ((l & 7) ^ ((l >> 2) & 4)) * 16;
      TIE(vdA);
    } else if constexpr (n == 9) {
      vdB = vdA + TV(T_DB); vdC = vdB + 16 * 896; vdA += TV(T_DA);
      TIE(vdA); TIE(vdB); TIE(vdC);
    } else if constexpr (n == 11) {
      fo1 = TV(T_F0) + (l & 15) * 8 - ((l >> 4) & 1) * TV(T_F1);
      TIE(fo1);
    } else if constexpr (n == 12) {
      rw2 = mkrs(w2p, TV(T_W2N));
    }
  };
  auto issueWh = [&](auto i_, auto hf_) AI {
    constexpr int i = i_, hf = hf_;
    unsigned& v = i < 8 ? vdA : i == 8 ? vdB : vdC;
    if constexpr (i == 18) { const unsigned v18 = vdB + TV(T_D18); dmab2c<wunit(i), B_18>(v18, v18 + (8 * 896 - 1024), rw2, lw2, TV(T_FLAGS)); }
    else if constexpr (hf == 0) { setm0<wunit(i)>(lw2); dmab<0>(v, rw2); v += 8 * 896 - 1024; TIE(v); }
    else { dmab<1024>(v, rw2); v += (i & 1) ? TV(T_STP) : 8 * 896 + 1024; TIE(v); }
  };
  auto issueW = [&](auto i) AI { issueWh(i, IC<0>{}); issueWh(i, IC<1>{}); };
  vf += 1536;
  auto item = [&](auto it_) AI {
    constexpr int it = it_;
    if constexpr (it < 35) itemA(it_);
    else if constexpr (it == 47 || it == 48 || it >= 51) issueWh(IC<(it >= 51)>{}, IC<((it - 47) & 1)>{});
    else {
      constexpr int h = it < 47 ? it - 35 : it - 37;
      if constexpr (!(h & 1)) setm0<bunit(h >> 1)>(lw2);
      dmag<(h & 1) * 1024>(vf, h >= 7 ? wu : wg);
      vf += (h == 6 ? -6 * 64 * 3584 : 64 * 3584) + ((h & 1) ? 1024 : -1024);
      TIE(vf);
    }
  };
  const unsigned odd = j & 1;

  bf16x8 xf[28];
  f32x16 acc[3], accB[2];
  f32x4 cs[3], csB[2], cp[3];
#pragma unroll
  for (int t = 0; t < 3; ++t) { acc[t] = 0.f; cs[t] = 0.f; }
#pragma unroll
  for (int t = 0; t < 2; ++t) { accB[t] = 0.f; csB[t] = 0.f; }
  bf16x8 wf[3][3], wb[6];
  auto rdX = [&](auto n_) AI {
    constexpr int n = n_, s = n >> 2, i = n & 3;
    if constexpr (s == 2 || s == 6) { bx[i] += 2 * SLOT; TIE(bx[i]); }
    if constexpr (s == 4) { bx[i] -= 2 * SLOT; TIE(bx[i]); }
    xf[n] = *(const LAS bf16x8*)(uintptr_t)(bx[i] + (s & 1) * SLOT);
  };
  auto rdW = [&](auto n_, auto t_) AI {
    constexpr int n = n_, t = t_, s = n >> 2, i = n & 3;
    if constexpr (t == 0 && (s == 2 || s == 6)) { bw[i] += 2 * SLOT; TIE(bw[i]); }
    if constexpr (t == 0 && s == 4) { bw[i] -= 2 * SLOT; TIE(bw[i]); }
    wf[n % 3][t] = ldtr(bw[i] + (s & 1) * SLOT + t * 1024);
  };
  auto rdB = [&](auto u_) AI {
    constexpr int u = u_, i = u & 3, h = u >> 2;
    constexpr unsigned base = h < 4 ? 8 * UNIT : h < 10 ? 0 : 4 * UNIT;
    if constexpr (h == 0) { bw[i] = awB ^ (i * 4096); TIE(bw[i]); }
    if constexpr (h == 4) { bw[i] -= 8 * UNIT; TIE(bw[i]); }
    if constexpr (h == 10) { bw[i] += 4 * UNIT; TIE(bw[i]); }
    constexpr unsigned off = bunit(h >> 1) - base + (h & 1) * 1024;
    wb[u % 6] = ldtr(bw[i] + off);
  };
  v4u stg;
  float rstd, sa = 0.f, sq = 0.f;
  bf16x8 mu, cf[3];

  asm volatile("s_waitcnt lgkmcnt(0)" : "+s"(tag), "+s"(ta), "+s"(tb)::"memory");
  {
    const unsigned L = umin(TV(T_WM) + umin(lane, TV(T_WMN)), TV(T_WMX));
    vwm = L * 128 + (wv < 4 ? (L >> 4) * 1536 + 1536 : 0u);
    TIE(vwm);
  }
  tag += TAG_STEP;
  u64 tagv = tag;
  TIE(tagv);
  STAMPP(8);
  sfor<PRO - 5>([&](auto it) AI { itemA(IC<5 + it>{}); });
  STAMPP(9);
  waitv<PRO - 5 + 2>();
  TIE(warm0);
  BAR();
  STAMP(1); CSTAMP(12);
  sfor<2>([&](auto n) AI { rdX(n); rdW(n, IC<0>{}); rdW(n, IC<1>{}); rdW(n, IC<2>{}); });
  sfor<28>([&](auto n_) AI {
    constexpr int n = n_, s = n >> 2, i = n & 3, c0 = cum(n - 1), c1 = cum(n);
    SCHED();
    if constexpr (i != 2) PRIO(3 - ((i + 2) & 3));
    if constexpr (i != 3 && n > 0) waitl<7>();
    acc[0] = mm32(wf[n % 3][0], xf[n], acc[0]);
    SCHED();
    if constexpr (i == 2) {
      if constexpr (s == 4) { if (tid < 4) *(LAS unsigned*)(uintptr_t)(L_CNT + tid * 4) = 0u; }
      if constexpr (s == 6) {
#pragma unroll
        for (int t = 0; t < 3; ++t) *(LAS float*)(uintptr_t)(aC + (w & 3) * 4 + t * 512) = cs[t][0];
      }
      waitl<0>();
      static_assert(c0 > (s < 6 ? 5 * s + 9 : 36), "next stage issued");
      constexpr int y = nops(n - 1) - 1 - rankI(s < 6 ? 5 * s + 9 : 36);
      waitv<s == 5 ? imin(y, nops(n - 1) - nops(G_ST)) : y>();
      if constexpr (s == 0) { TIE(warm); TIE(xrow); }
      if constexpr (s == 1) { static_assert(rankI(14) >= nops(1), "covered"); TIE(warm2); }
#pragma unroll
      for (int t = 0; t < 3; ++t) { TIE(acc[t]); TIE(cs[t]); }
      BAR();
      PRIO(3);
      if constexpr (s == 6) {
#pragma unroll
        for (int t = 0; t < 3; ++t) cp[t] = *(const LAS f32x4*)(uintptr_t)(aC + t * 512);
      }
    }
    if constexpr (n < 26) {
      rdX(IC<n + 2>{});
      rdW(IC<n + 2>{}, IC<0>{});
      if constexpr (i == 1) rdW(IC<n + 2>{}, IC<1>{});
    } else rdB(IC<2 * (n - 26)>{});
    if constexpr (n == 2 || n == 3) {
#pragma unroll
      for (int e = 2 * (n - 2); e < 2 * (n - 2) + 2; ++e) {
        const unsigned d = lane < 56 ? xrow[e] : 0u;
        const float lo = blo(d), hi = bhi(d);
        sa += lo + hi; sq = __builtin_fmaf(lo, lo, __builtin_fmaf(hi, hi, sq));
      }
      TIE(sa); TIE(sq);
    }
    if constexpr (n == 6) { sa = wave_sum63(sa); sq = wave_sum63(sq); TIE(sa); TIE(sq); }
    if constexpr (n >= 4 && n <= 12 && i != 2) setup(n_);
    if constexpr (n == 23) {
      TIE(stg);
      pollg(stg, oS, rs, tag);
      rstd = asf(stg[2]);
      const float m = asf(stg[0]);
      const unsigned mh = pk(-m, -m);
      const float ml = -m - blo(mh);
      mu = asb(v4u{hh ? 0u : mh, hh ? 0u : pk(ml, ml), 0u, 0u});
      TIE(mu); TIE(rstd);
    }
    if constexpr (n == 27) { cf[1] = csfrag(cp[1]); TIE(cf[1]); }
    SCHED();
    if constexpr (i == 0) cs[0] = mm16(sel, wf[n % 3][0], cs[0]);
    if constexpr (n == 27) acc[0] = mm32(cf[0], mu, acc[0]);
    acc[1] = mm32(wf[n % 3][1], xf[n], acc[1]);
    SCHED();
    if constexpr (n < 26) rdW(IC<n + 2>{}, IC<(i == 1 ? 2 : 1)>{}); else rdB(IC<2 * (n - 26) + 1>{});
    if constexpr (c1 > c0) item(IC<c0>{});
    if constexpr (n == 27) { cf[2] = csfrag(cp[2]); TIE(cf[2]); }
    SCHED();
    if constexpr (i == 0) cs[1] = mm16(sel, wf[n % 3][1], cs[1]);
    if constexpr (n == 27) acc[1] = mm32(cf[1], mu, acc[1]);
    acc[2] = mm32(wf[n % 3][2], xf[n], acc[2]);
    SCHED();
    if constexpr (i != 1 && n < 26) rdW(IC<n + 2>{}, IC<2>{});
    if constexpr (c1 > c0 + 1) item(IC<c0 + 1>{});
    if constexpr (n == 1) touch(warm2, vwm, bwm);
    if constexpr (n == 7) {
      const float m = sa * (1.0f / 448.0f);
      const float r = __builtin_amdgcn_rsqf(__builtin_fmaxf(sq * (1.0f / 448.0f) - m * m, 0.f) + 1e-5f);
      const v4u gr = {asu(m), (unsigned)tagv, asu(r), (unsigned)(tagv >> 32)};
      __builtin_amdgcn_raw_buffer_store_b128(gr, rs, lane == 63 ? WS_ST + ts * 16 : OOB, 0, AUX_SC1);
    }
    if constexpr (n == G_ST) ld16(stg, oS, rs);
    if constexpr (n == 26) { cf[0] = csfrag(cp[0]); TIE(cf[0]); }
    SCHED();
    if constexpr (i == 0) cs[2] = mm16(sel, wf[n % 3][2], cs[2]);
    if constexpr (n == 27) acc[2] = mm32(cf[2], mu, acc[2]);
  });
  SCHED();
  STAMP(2); CSTAMP(13);
  u64 fl;
  v4u qf, k0, k1, v0, v1, hv;
  bf16x8 w2[9][2];
  float sg[16];
  const float k1s = -1.4426950408889634f * rstd, ik2 = __builtin_amdgcn_rcpf(rstd * rstd);
  auto rdW2 = [&](auto i_, bf16x8* d) AI {
    constexpr unsigned U = wunit(decltype(i_)::value);
    unsigned c = a2 + U, e = c ^ 64;
    TIE(c); TIE(e);
    d[0] = ldtr(c); d[1] = ldtr(e);
  };
  const unsigned oQ = vl + TV(T_Q), fl_ = TV(T_FLAGS), fme = TV(T_ME);
  unsigned hd[4];
  float hprev;
  sfor<56>([&](auto u_) AI {
    constexpr int u = u_, t = u >= 28, k = u % 28;
    SCHED();
    accB[t] = mm32(wb[u % 6], xf[k], accB[t]);
    SCHED();
    if constexpr ((u & 7) == 4) {
      if constexpr (u == 28 || u == 52) *(LAS float*)(uintptr_t)(aC + (w & 3) * 4 + (u == 52) * 512) = csB[u == 52][0];
      waitl<0>();
      if constexpr (u == U_F1) STAMPB(6);
      if constexpr (u == 4) waitv<nops(27) - 1 - rankI(48) + bsum(0, u)>();
      if constexpr (u == 12) waitc<bsum(0, u) + 2, bsum(0, u), B_U12>(fl_);
      if constexpr (u == U_F1) waitv<bsum(8, u)>();
      if constexpr (u == U_FE) waitv<bsum(20, u)>();
      if constexpr (u == 36 || u == 52) waitv<bsum(bslot(2 * breg(u) + 1) + 1, u)>();
      if constexpr (u == U_ATT) { STAMPB(8); poll<bsum(U_POLL + 1, U_ATT), B_7>(fl, fo1, rs, tag, fl_); }
      TIE(accB[t]); TIE(csB[t]);
      BAR();
      if constexpr (u == 12) STAMPB(5);
      if constexpr (u == U_ATT) STAMPB(9);
      if constexpr (u == U_F1) STAMPB(7);
      if constexpr (u == U_F1 || u == U_FE) raise<B_6>(fme + (u == U_F1 ? 0 : WS_FE - WS_F1), rs, tagv, fl_);
      if constexpr (u == 28 || u == 52) cp[0] = *(const LAS f32x4*)(uintptr_t)(aC + (u == 52) * 512);
    }
    if constexpr ((u & 1) == 0) PRIO(3 - (((u + 4) >> 1) & 3));
    if constexpr (breg(u) >= 0) { constexpr int R = breg(u); rdW2(IC<R>{}, w2[R]); SCHED(); }
    if constexpr (u + 4 < 56) rdB(IC<u + 4>{});
    if constexpr (u < 4) {
      hd[2 * (u & 1)] = pk(acc[0][4 * u] * rstd, acc[0][4 * u + 1] * rstd);
      hd[2 * (u & 1) + 1] = pk(acc[0][4 * u + 2] * rstd, acc[0][4 * u + 3] * rstd);
      if constexpr (u & 1) __builtin_amdgcn_raw_buffer_store_b128(v4u{hd[0], hd[1], hd[2], hd[3]}, rs, oQ + (u >> 1) * 1024, 0, AUX_SC1);
      else { TIE(hd[0]); TIE(hd[1]); }
    } else if constexpr (u < 20) {
      constexpr int e = u - 4;
      if constexpr (e < 4) {
        if (!typeA) {
          constexpr int T = 1 + (e >> 1), f = 8 * (e & 1);
          v4u o;
#pragma unroll
          for (int d = 0; d < 4; ++d) o[d] = pk(acc[T][f + 2 * d] * rstd, acc[T][f + 2 * d + 1] * rstd);
          __builtin_amdgcn_raw_buffer_store_b128(o, rs, oQ + T * 4096 + (e & 1) * 1024, 0, AUX_SC1);
        }
      }
      const float hval = silu_k(acc[1][e], k1s, ik2) * acc[2][e];
      if constexpr (e & 1) { hd[(e >> 1) & 3] = pk(hprev, hval); TIE(hd[(e >> 1) & 3]); }
      else { hprev = hval; TIE(hprev); }
      if constexpr ((e & 7) == 7) __builtin_amdgcn_raw_buffer_store_b128(v4u{hd[0], hd[1], hd[2], hd[3]}, rs, vl + TV(T_E) + (e >> 3) * 1024, 0, AUX_SC1);
    }
    if constexpr (u == 29 || u == 53) { cf[0] = csfrag(cp[0]); TIE(cf[0]); }
    if constexpr (u >= 32 && u < 52 && (u - 32) % 5 != 4) {
      constexpr int e = (u - 32) - (u - 32) / 5;
      sg[e] = silu_k(accB[0][e], k1s, ik2);
      TIE(sg[e]);
    }
    if constexpr (u == U_ATT) { ld16s(qf, la, rs, TV(T_AQ)); ld16s(k0, la, rs, TV(T_K0)); }
    if constexpr (u == U_ATT + 1) { ld16s(k1, la, rs, TV(T_K1)); ld16s(v0, la, rs, TV(T_V0)); }
    if constexpr (u == U_ATT + 2) ld16s(v1, la, rs, TV(T_V1));
    if constexpr (bw2(u) >= 0) issueWh(IC<(bw2(u) >> 1)>{}, IC<(bw2(u) & 1)>{});
    if constexpr (u == U_POLL) ld8c<B_7>(fl, fo1, rs, fl_);
    if constexpr (u == 55) ld8c<B_6>(fl, fo1, rs, fl_);
    SCHED();
    if constexpr (u == 0) csB[0] = mm16(sel, wb[0], csB[0]);
    if constexpr (u + 2 < 56 && ((u + 2) & 3) == 0) csB[u + 2 >= 28] = mm16(sel, wb[(u + 2) % 6], csB[u + 2 >= 28]);
    if constexpr (u == 30 || u == 54) accB[u == 54] = mm32(cf[0], mu, accB[u == 54]);
  });
  SCHED();
  PRIO(3);
  STAMP(4); CSTAMP(14);
#pragma unroll
  for (int c = 0; c < 2; ++c) {
#pragma unroll
    for (int e = 0; e < 4; ++e) hv[e] = pk(sg[8 * c + 2 * e] * accB[1][8 * c + 2 * e], sg[8 * c + 2 * e + 1] * accB[1][8 * c + 2 * e + 1]);
    __builtin_amdgcn_raw_buffer_store_b128(hv, rs, vl + TV(T_H) + 24 * 2048 + c * 1024, 0, AUX_SC1);
  }
  SCHED();
  STAMPN(5);
  unsigned lane_ = __builtin_amdgcn_mbcnt_hi(~0u, __builtin_amdgcn_mbcnt_lo(~0u, 0u));
  TIE(lane_);
  const unsigned n16_ = lane_ & 15, g4_ = lane_ >> 4, q4_ = n16_ >> 2, p4_ = lane_ & 3, tok_ = lane_ & 31, hh_ = lane_ >> 5, l7_ = lane_ & 7;
  const unsigned vA = vl + TV(T_A), vB = vl + TV(T_B);
  unsigned sT = HO_T;
  TIES(sT);
  v4u af[19][2];
  auto ldE2 = [&](auto i_) AI {
    constexpr int i = i_;
    ld16x4c<(i & 3) * 1024, B_H>(af[i][0], af[i][1], af[i + 1][0], af[i + 1][1], vA + (i >> 2) * 4096, rs, sT, fl_);
  };
  auto ldL = [&](auto i_) AI {
    constexpr int i = i_;
    if constexpr (i == 18) ld16x2c<(i & 3) * 1024, B_18>(af[i][0], af[i][1], vB + ((i - 8) >> 2) * 4096, rs, sT, fl_);
    else ld16x2<(i & 3) * 1024>(af[i][0], af[i][1], vB + ((i - 8) >> 2) * 4096, rs, sT);
  };
  waitv<bsum(U_ATT + 3, 56) + 2>();
  TIE(qf); TIE(k0); TIE(k1); TIE(v0); TIE(v1);
  {
    if (odd && g4_ == 0) { qf[0] = 0; qf[1] = 0; }
    if (!odd && g4_ == 3) { qf[2] = 0; qf[3] = 0; }
    constexpr unsigned U4 = wunit(4);
    const unsigned scr = U4 + w * 2048;
    *(LAS v4u*)(uintptr_t)(scr + n16_ * 64 + g4_ * 16) = v0;
    *(LAS v4u*)(uintptr_t)(scr + 1024 + n16_ * 64 + g4_ * 16) = v1;
    const f32x4 z4 = 0.f;
    const f32x4 sc0 = mm16(asb(k0), asb(qf), z4), sc1 = mm16(asb(k1), asb(qf), z4);
    bf16x8 vt[2];
#pragma unroll
    for (int t = 0; t < 2; ++t) {
      const unsigned a = scr + (4 * g4_ + q4_) * 64 + (((2 * p4_ + t + odd) * 8) & 63);
      const v4s lo = __builtin_amdgcn_ds_read_tr16_b64_v4i16((LAS v4s*)(uintptr_t)a);
      const v4s hi = __builtin_amdgcn_ds_read_tr16_b64_v4i16((LAS v4s*)(uintptr_t)(a + 1024));
      vt[t] = __builtin_shufflevector(lo, hi, 0, 1, 2, 3, 4, 5, 6, 7);
    }
    const bool par = n16_ & 1, c0 = n16_ >= 4 * g4_, c1 = n16_ >= 4 * g4_ + 2, back = (p | w) != 0;
    const float ninf = -3.0e38f, sc = 0.1889822365046136f * 1.4426950408889634f;
    const float x0 = par ? sc0[1] : sc0[0], x1 = par ? sc0[3] : sc0[2], y0 = par ? sc1[1] : sc1[0], y1 = par ? sc1[3] : sc1[2];
    float s0 = (c0 ? y0 : x0) * sc, s1 = (c1 ? y1 : x1) * sc;
    s0 = (c0 || back) ? s0 : ninf; s1 = (c1 || back) ? s1 : ninf;
    const float mx = rows4<true>(__builtin_fmaxf(s0, s1));
    const float e0 = __builtin_amdgcn_exp2f(s0 - mx), e1 = __builtin_amdgcn_exp2f(s1 - mx);
    const float inv = __builtin_amdgcn_rcpf(rows4<false>(e0 + e1));
    const unsigned pe = pk(e0 * inv, e1 * inv);
    const unsigned u0 = par ? pe << 16 : pe & 0xffffu, u1 = par ? pe & 0xffff0000u : pe >> 16;
    const v4u pf = {c0 ? 0u : u0, c1 ? 0u : u1, c0 ? u0 : 0u, c1 ? u1 : 0u};
    const f32x4 oa = mm16(vt[0], asb(pf), z4), ob = mm16(vt[1], asb(pf), z4);
    v4u ov = {pk(oa[0], oa[1]), pk(oa[2], oa[3]), pk(ob[0], ob[1]), pk(ob[2], ob[3])};
    if (g4_ == 3) { ov[2] = 0; ov[3] = 0; }
    __builtin_amdgcn_raw_buffer_store_b128(ov, rs, la + TV(T_O), 0, AUX_SC1);
  }
  SCHED();
  STAMPN(6);
  waitv<3>();
  sfor<4>([&](auto i) AI { rdW2(IC<5 + i>{}, w2[5 + i]); });
  poll<3, B_6>(fl, fo1, rs, tag, fl_);
  if (w == 6) *(LAS unsigned*)(uintptr_t)(L_CNT + 12) = 1u;
  waitw(L_CNT + 12, 1u);
  sfor<NPRE>([&](auto i) AI { ldE2(IC<2 * i>{}); });
  waitc<4 * NPRE, 0, B_H>(fl_);
  arrive(L_CNT, fme + (WS_F2 - WS_F1), rs, tagv);
  STAMPN(7);
  if (!owave) PRIO(0);
  if (!p2) {
    if (b == 255 && w == 0) {
#pragma unroll 1
      for (int k = 0; k < 4; ++k) poll<0, 0>(~tag, WS_F1 + (k * 4 + g4_) * FSTR + n16_ * 8, rs, tag, 1u);
      raise<0>(0u, rs, tagv, 1u);
    }
    return;
  }

  {
    const unsigned fo2 = n16_ * 8 + TV(T_F2);
    const unsigned vo = (lane_ >> 3) * 896 + l7_ * 16 + TV(T_OUT);
    v4u xres;
    f32x16 c2[2][2];
    bf16x8 wl[2][2];
#pragma unroll
    for (int t = 0; t < 4; ++t) c2[t >> 1][t & 1] = 0.f;
    auto step = [&](auto i_) AI {
      constexpr int i = i_;
      const bf16x8* wp = i < 9 ? w2[i < 9 ? i : 0] : wl[i & 1];
      if constexpr (i >= 8 && i < 18) { rdW2(IC<i + 1>{}, wl[(i + 1) & 1]); SCHED(); }
      TIE(af[i][0]); TIE(af[i][1]);
      c2[0][0] = mm32(wp[0], asb(af[i][0]), c2[0][0]);
      c2[0][1] = mm32(wp[0], asb(af[i][1]), c2[0][1]);
      c2[1][0] = mm32(wp[1], asb(af[i][0]), c2[1][0]);
      c2[1][1] = mm32(wp[1], asb(af[i][1]), c2[1][1]);
      SCHED();
    };
    sfor<4 - NPRE>([&](auto i) AI { ldE2(IC<2 * (NPRE + i)>{}); });
    sfor<18 - WREST>([&](auto i) AI { issueW(IC<WREST + i>{}); });
    asm volatile("global_load_dwordx4 %0, %1, %2" : "=v"(xres) : "v"(vo), "s"(x) : "memory");
    if (!owave) { waitv<10 + 2 * (18 - WREST) + 1>(); step(IC<0>{}); step(IC<1>{}); step(IC<2>{}); }
    if (owave) watch(fo2, rs, tag, L_CNT + 8, TV(T_NEED)); else waitw(L_CNT + 8, TV(T_NEED));
    PRIO(0);
    STAMPN(8);
    ld16x10r(af[3], af[4], af[5], af[6], af[7], vA, vA + 4096, rs, sT, fl_);
    sfor<5>([&](auto i) AI { ldL(IC<8 + i>{}); });
    sfor<5>([&](auto i) AI {
      waitv<18>();
      step(IC<3 + i>{});
      ldL(IC<13 + i>{});
    });
    ldL(IC<18>{});
    sfor<10>([&](auto i) AI { waitv<2 * (9 - i)>(); step(IC<8 + i>{}); });
    if (w < 4 || owave) step(IC<18>{});
    STAMPN(9);
#pragma unroll
    for (int nt = 0; nt < 2; ++nt)
#pragma unroll
      for (int tt = 0; tt < 2; ++tt)
#pragma unroll
        for (int c = 0; c < 2; ++c) {
          v4u o;
#pragma unroll
          for (int e = 0; e < 4; ++e) o[e] = pk(c2[nt][tt][8 * c + 2 * e], c2[nt][tt][8 * c + 2 * e + 1]);
          *(LAS v4u*)(uintptr_t)((2 * nt + c) * UNIT + w * 2048 + hh_ * 1024 + (((32 * tt + tok_) ^ (4 * (2 * c + hh_))) * 16)) = o;
        }
    WAITL0();
    BAR();
    STAMP(10);
    float f[8], sv[8];
    const unsigned ar = (l7_ >> 1) * UNIT + (l7_ & 1) * 1024 + (((8 * w + (lane_ >> 3)) ^ (4 * (l7_ & 3))) * 16);
#pragma unroll
    for (int k = 0; k < 8; ++k) {
      const v4u v = *(const LAS v4u*)(uintptr_t)(ar + k * 2048);
#pragma unroll
      for (int e = 0; e < 4; ++e) {
        if (k == 0) { f[2 * e] = blo(v[e]); f[2 * e + 1] = bhi(v[e]); }
        else if (k < 6) { f[2 * e] += blo(v[e]); f[2 * e + 1] += bhi(v[e]); }
        else if (k == 6) { sv[2 * e] = blo(v[e]); sv[2 * e + 1] = bhi(v[e]); }
        else { sv[2 * e] += blo(v[e]); sv[2 * e + 1] += bhi(v[e]); }
      }
    }
    TIE(xres);
    float r[8];
#pragma unroll
    for (int e = 0; e < 8; ++e) {
      const float g = f[e] * __builtin_amdgcn_rcpf(1.0f + __builtin_amdgcn_exp2f(f[e] * -1.4426950408889634f));
      r[e] = __builtin_fmaf(sv[e], g, (e & 1) ? bhi(xres[e >> 1]) : blo(xres[e >> 1]));
    }
    const v4u o = {pk(r[0], r[1]), pk(r[2], r[3]), pk(r[4], r[5]), pk(r[6], r[7])};
    *(GAS v4u*)(out + vo) = o;
    STAMP(11); CSTAMP(15);
    while (__builtin_amdgcn_s_memrealtime() - est[0] < 6000 + 10 * (est[1] - est[0])) __builtin_amdgcn_s_sleep(1);
  }
}

extern "C" void kernel_launch(void* const* d_in, const int* in_sizes, int n_in, void* d_out, int out_size, void* d_ws, size_t ws_size,
                              hipStream_t stream) {
  (void)in_sizes; (void)n_in; (void)out_size; (void)ws_size;
  (void)hipFuncSetAttribute((const void*)fused_block, hipFuncAttributeMaxDynamicSharedMemorySize, 163840);
  fused_block<<<256, 512, 163840, stream>>>((const char*)d_in[0], (const char*)d_in[1], (const char*)d_in[2], (const char*)d_in[4],
                                            (const char*)d_in[5], (const char*)d_in[6], (char*)d_out, (char*)d_ws);
}
```

```cpp
#include <hip/hip_runtime.h>
#include <stdint.h>
#include <utility>

#define GAS __attribute__((address_space(1)))
#define LAS __attribute__((address_space(3)))
#define AI __attribute__((always_inline))
typedef unsigned v4u __attribute__((ext_vector_type(4)));
typedef short v4s __attribute__((ext_vector_type(4)));
typedef short bf16x8 __attribute__((ext_vector_type(8)));
typedef float f32x4 __attribute__((ext_vector_type(4)));
typedef float f32x16 __attribute__((ext_vector_type(16)));
typedef float v2f __attribute__((ext_vector_type(2)));
typedef __bf16 v2bf __attribute__((ext_vector_type(2)));
typedef unsigned long long u64;

namespace {
constexpr unsigned FSTR = 4224;
constexpr unsigned WS_F1 = 65536, WS_FE = WS_F1 + 16 * FSTR, WS_F2 = WS_FE + 16 * FSTR, WS_F3 = WS_F2 + 16 * FSTR;
constexpr unsigned WS_ST = 32768;
constexpr unsigned WS_QKV = 393216, QKV_T = 168 * 512;
constexpr unsigned WS_HO = WS_QKV + 64 * QKV_T, HO_T = 288 * 512;
constexpr unsigned WS_END = WS_HO + 64 * HO_T;
constexpr unsigned WS_DBG = 32u << 20;
constexpr u64 TAG_STEP = 0x9E3779B97F4A7C15ull;
constexpr int AUX_SC1 = 16;
constexpr unsigned SLOT = 40960, UNIT = 16384;
constexpr unsigned L_CSA = 122880, L_CNT = L_CSA + 3072;
constexpr unsigned CODE_BYTES = 23552;
constexpr unsigned OOB = 0x40000000u;
constexpr unsigned bunit(int s) { return (s == 0 ? 8 : s == 1 ? 9 : s == 2 ? 0 : s == 3 ? 1 : s == 4 ? 3 : s == 5 ? 4 : 5) * UNIT; }
constexpr unsigned wunit(int i) {
  return i == 18 ? 7 * UNIT : (i % 9) == 0 ? 2 * UNIT : (i % 9) == 1 ? 6 * UNIT : bunit(i % 9 - 2);
}
#define UPOLL 46
#define GW2 1
#define FAST 0
#define KROT 1
constexpr int PRO = 10, G_ST = 18;
constexpr int avail(int g) {
  return g < 2 ? 20 : g < 6 ? 25 : g < 10 ? 30 : g < 14 ? 35 : g < 18 ? 39 : g < 22 ? 43 : g < 26 ? 49 : 53;
}
constexpr int cum(int g) {
  int c = PRO;
  for (int k = 0; k <= g; ++k) {
    int n = c + ((k & 1) && k != 27 && k >= FAST ? 1 : 2);
    c = n < avail(k) ? n : avail(k);
  }
  return c;
}
static_assert(cum(27) == 53, "every item issued");
constexpr int nextra(int g) { return (g == GW2) + (g == 9) + (g == G_ST); }
constexpr int nops(int g) {
  int c = PRO + 3;
  for (int k = 0; k <= g; ++k) c += cum(k) - cum(k - 1) + nextra(k);
  return c;
}
constexpr int rankI(int it) {
  if (it < PRO) return it < 5 ? it + 1 : it + 3;
  int g = 0;
  while (cum(g) <= it) ++g;
  return nops(g - 1) + it - cum(g - 1);
}
__device__ __forceinline__ unsigned umin(unsigned a, unsigned b) { return a < b ? a : b; }
constexpr int imin(int a, int b) { return a < b ? a : b; }
constexpr int U_F1 = 28, U_FE = 44, U_POLL = UPOLL, U_ATT = 52, U_S2 = 15, U_S3 = 27;
constexpr bool bbar(int u) { return u == 12 || u == 28 || u == 44 || u == 52; }
constexpr int bw2(int u) {
  return u == 9 ? 36 : u == 13 ? 4 : u == 14 ? 5 : u == 18 ? 6 : u == 20 ? 7 : u == 22 ? 18 : u == 24 ? 19 : u == 30 ? 8 : u == 32 ? 9
       : u == 34 ? 10 : u == 36 ? 11 : u == 39 ? 20 : u == 41 ? 21 : u == 44 ? 12 : u == 45 ? 13 : u == 48 ? 14 : u == 50 ? 15
       : u == 54 ? 16 : u == 55 ? 17 : -1;
}
constexpr int breg(int u) { return u == 12 ? 0 : u == 17 ? 1 : u == 52 ? 2 : -1; }
constexpr int hfirst(int v) { return v <= 0 ? 0 : v >= 24 ? 32 : (v / 3) * 4 + (v % 3); }
constexpr int bops(int u) {
  const int p = bw2(u) >> 1;
  return (u == 1 || u == 3 || u == U_S2 || u == U_S3) + (bw2(u) >= 0 && p != 18) + (u == U_ATT || u == U_ATT + 1 ? 2 : u == U_ATT + 2 ? 1 : 0);
}
constexpr int bsum(int u0, int u1) { int c = 0; for (int u = u0; u < u1; ++u) c += bops(u); return c; }
constexpr int bslot(int piece) { int u = 0; while (bw2(u) != piece) ++u; return u; }

#define WIN 6
#define RAMP 3
#define P2W 12
#define OPRI 0
#define OUP 99
#define HPRE 3
#define OPRE 0
constexpr int p2req(int i) { return i < 0 ? WIN : i < P2W ? imin(WIN + 2 * i, 12) : imin(imin(12 + RAMP + 2 * (i - P2W), i + 11), 28); }
constexpr int p2piece(int i) { return i == 9 ? 32 + 8 : i == 10 ? 32 + 0 : i == 11 ? 32 + 1 : i < 14 ? i - 12 : i == 14 ? 18 : i < 21 ? 32 + i - 13 : i - 19; }

enum { T_Q, T_H, T_E, T_S, T_AQ, T_K0, T_K1, T_V0, T_V1, T_DA, T_DB, T_DC, T_F0, T_F1, T_ME, T_FLAGS,
       T_A, T_B, T_OUT, T_F2, T_FE, T_O, T_STP, T_CS, T_WM, T_WMN, T_WMX, T_W2N, T_AWB, T_DT, T_N = 32 };
enum { B_H, B_18, B_U12, B_7, B_6 };
struct Tab {
  unsigned v[2048][T_N];
  constexpr Tab() : v() {
    for (unsigned b = 0; b < 256; ++b)
      for (unsigned w = 0; w < 8; ++w) {
        unsigned* t = v[b * 8 + w];
        const unsigned xl = b & 7, idx = b >> 3, rg = w & 3, set = w >> 2;
        const bool typeA = (idx & 3) < 3, p2 = idx < 28, ow = w >= 4;
        const unsigned j = typeA ? 3 * (xl & 3) + (idx & 3) : 12 + (xl & 3), p = 8 * (xl >> 2) + (idx >> 2);
        const unsigned qb0 = typeA ? j : j == 15 ? 18 : 3 * j - 24, tt1 = 4 * p + rg;
        const unsigned rg2 = p2 ? 4 * xl + idx / 7 : 2 * p, cg = idx % 7;
        const unsigned q = KROT ? (w + cg + idx / 7) & 3 : w & 3;
        t[T_Q] = j < 15 ? WS_QKV + tt1 * QKV_T + (2 * qb0 + set) * 2048 : OOB;
        t[T_H] = WS_HO + tt1 * HO_T + (2 * j + set) * 2048;
        t[T_E] = typeA ? t[T_H] : OOB;
        t[T_S] = WS_ST + tt1 * 512;
        const unsigned aq = WS_QKV + (4 * p + (w >> 1)) * QKV_T + (w & 1) * 256 + ((7 * j) >> 1) * 512;
        const unsigned ak = aq - ((w & 1) ? 256u : (p | w) ? QKV_T - 256u : 0u);
        t[T_AQ] = aq; t[T_K0] = ak + 56 * 512; t[T_K1] = aq + 56 * 512; t[T_V0] = ak + 112 * 512; t[T_V1] = aq + 112 * 512;
        t[T_O] = WS_HO + (4 * p + (w >> 1)) * HO_T + (w & 1) * 256 + (224 + 4 * j) * 512;
        t[T_DA] = p2 ? cg * 128 + (ow ? 112 * q : 192 * q) * 896 : OOB;
        t[T_DB] = p2 ? cg * 128 + (192 * q + (ow ? 144 : 128)) * 896 : OOB;
        t[T_DC] = p2 ? cg * 128 + (ow ? 192 * q + 160 : 768 + 256 * q) * 896 : OOB;
        t[T_DT] = cg * 128 + (768 + 256 * q + 48) * 896;
        t[T_STP] = ow ? 4 * 896 + 1024 : 8 * 896 + 1024;
        t[T_W2N] = ow ? 448 * 896 : 1792 * 896;
        t[T_A] = WS_HO + 2 * rg2 * HO_T + 12 * q * 1024;
        t[T_B] = WS_HO + 2 * rg2 * HO_T + (ow ? 112 + 8 * q : 48 + 16 * q) * 1024;
        t[T_OUT] = (64 * rg2 + 16 * (w >> 1)) * 896 + cg * 128 + (w & 1) * 64;
        t[T_F0] = ow ? WS_F1 + p * FSTR : WS_FE + (rg2 >> 1) * FSTR + 3 * q * 8;
        t[T_F1] = ow && p > 0 ? FSTR : 0;
        t[T_F2] = WS_F2 + (rg2 >> 1) * FSTR + 4 * q * 8;
        t[T_FE] = ow ? 15 : 2;
        t[T_ME] = WS_F1 + p * FSTR + j * 8;
        t[T_FLAGS] = (p2 && !ow) << B_H | !ow << B_18 | (!ow || !typeA) << B_U12 | (w == 7) << B_7 | (w == 6) << B_6;
        t[T_CS] = L_CSA + set * 1536;
        t[T_AWB] = 8 * UNIT - 2 * (rg ^ (2 * set)) * 1024;
        const unsigned n = w < 4 ? 14 : w < 7 ? 17 : 13;
        t[T_WM] = (w < 4 ? b * 2 + (w & 1) : w < 7 ? b * 3 + w - 4 : b) * n; t[T_WMN] = n - 1; t[T_WMX] = w < 7 ? 12543u : 3135u;
      }
  }
};
__constant__ const Tab kTab = Tab();
typedef unsigned v16u __attribute__((ext_vector_type(16)));

template <int N> using IC = std::integral_constant<int, N>;
template <int... I, class F> __device__ __forceinline__ void sfor_(std::integer_sequence<int, I...>, F&& f) { (f(std::integral_constant<int, I>{}), ...); }
template <int N, class F> __device__ __forceinline__ void sfor(F&& f) { sfor_(std::make_integer_sequence<int, N>{}, f); }

#define WAITL0() asm volatile("s_waitcnt lgkmcnt(0)" ::: "memory")
#define BAR() do { asm volatile("" ::: "memory"); __builtin_amdgcn_s_barrier(); asm volatile("" ::: "memory"); } while (0)
#define SCHED() __builtin_amdgcn_sched_barrier(0)
#define PRIO(n) __builtin_amdgcn_s_setprio(n)
template <class T> __device__ __forceinline__ void TIE(T& v) { asm volatile("" : "+v"(v)); }
template <class T> __device__ __forceinline__ void TIES(T& v) { asm volatile("" : "+s"(v)); }
template <int N> __device__ __forceinline__ void waitv() { asm volatile("s_waitcnt vmcnt(%0)" ::"i"(N) : "memory"); }
template <int N> __device__ __forceinline__ void waitl() { __builtin_amdgcn_s_waitcnt(0xC07F | (N << 8)); }

typedef __amdgpu_buffer_rsrc_t rsrc_t;
__device__ __forceinline__ rsrc_t mkrs(const char* p, unsigned bytes) {
  rsrc_t r = __builtin_amdgcn_make_buffer_rsrc((void*)p, 0, bytes, 0x00020000);
  TIES(r);
  return r;
}
template <unsigned L> __device__ __forceinline__ void setm0(unsigned ldsw) {
  asm volatile("s_add_u32 m0, %0, %1\n\ts_nop 0" ::"s"(ldsw), "i"(L) : "memory", "scc");
}
template <unsigned O> __device__ __forceinline__ void dmag(unsigned voff, const char* base) {
  asm volatile("global_load_lds_dwordx4 %0, %1 offset:%2" ::"v"(voff), "s"(base), "i"(O) : "memory");
}
template <unsigned O> __device__ __forceinline__ void dmab(unsigned voff, rsrc_t rs) {
  asm volatile("buffer_load_dwordx4 %0, %1, 0 offen offset:%2 lds" ::"v"(voff), "s"(rs), "i"(O) : "memory");
}
template <unsigned L, int B> __device__ __forceinline__ void dmab2c(unsigned v0, unsigned v1, rsrc_t rs, unsigned ldsw, unsigned c) {
  asm volatile("s_bitcmp0_b32 %5, %6\n\ts_cbranch_scc1 1f\n\ts_add_u32 m0, %3, %4\n\ts_nop 0\n\tbuffer_load_dwordx4 %0, %2, 0 offen lds\n\t"
               "buffer_load_dwordx4 %1, %2, 0 offen offset:1024 lds\n1:" ::"v"(v0), "v"(v1), "s"(rs), "s"(ldsw), "i"(L), "s"(c), "i"(B) : "memory", "scc");
}
__device__ __forceinline__ void ld16(v4u& d, unsigned voff, rsrc_t rs) {
  asm volatile("buffer_load_dwordx4 %0, %1, %2, 0 offen sc1" : "=v"(d) : "v"(voff), "s"(rs) : "memory");
}
__device__ __forceinline__ void ld16s(v4u& d, unsigned voff, rsrc_t rs, unsigned so) {
  asm volatile("buffer_load_dwordx4 %0, %1, %2, %3 offen sc1" : "=v"(d) : "v"(voff), "s"(rs), "s"(so) : "memory");
}
template <unsigned O> __device__ __forceinline__ void ld16x2(v4u& d0, v4u& d1, unsigned v, rsrc_t rs, unsigned so) {
  asm volatile("buffer_load_dwordx4 %0, %2, %3, 0 offen offset:%5 sc1\n\tbuffer_load_dwordx4 %1, %2, %3, %4 offen offset:%5 sc1"
               : "=&v"(d0), "=&v"(d1) : "v"(v), "s"(rs), "s"(so), "i"(O) : "memory");
}
__device__ __forceinline__ void touch(unsigned& d, unsigned voff, const char* base) {
  asm volatile("global_load_dword %0, %1, %2" : "=v"(d) : "v"(voff), "s"(base) : "memory");
}
template <int N1, int N0, int B> __device__ __forceinline__ void waitc(unsigned c) {
  static_assert(N0 <= N1 && N0 >= 0, "");
  asm volatile("s_bitcmp1_b32 %0, %3\n\ts_cbranch_scc1 1f\n\ts_waitcnt vmcnt(%2)\n1:\n\ts_waitcnt vmcnt(%1)" ::"s"(c), "i"(N1), "i"(N0), "i"(B) : "memory", "scc");
}
template <int S> __device__ __forceinline__ float wave_sum2(float v) {
#define DPP_ADD(ctrl, rm) v += __builtin_bit_cast(float, __builtin_amdgcn_update_dpp(0, __builtin_bit_cast(int, v), ctrl, rm, 0xf, true));
  if constexpr (S == 0) { DPP_ADD(0x111, 0xf) DPP_ADD(0x112, 0xf) }
  else if constexpr (S == 1) { DPP_ADD(0x114, 0xf) DPP_ADD(0x118, 0xf) }
  else { DPP_ADD(0x142, 0xa) DPP_ADD(0x143, 0xc) }
#undef DPP_ADD
  return v;
}
__device__ __forceinline__ bf16x8 ldtr(unsigned a) {
  v4s lo = __builtin_amdgcn_ds_read_tr16_b64_v4i16((LAS v4s*)(uintptr_t)a);
  v4s hi = __builtin_amdgcn_ds_read_tr16_b64_v4i16((LAS v4s*)(uintptr_t)(a + 512));
  return __builtin_shufflevector(lo, hi, 0, 1, 2, 3, 4, 5, 6, 7);
}
__device__ __forceinline__ unsigned pk(float a, float b) {
  v2f f = {a, b};
  return __builtin_bit_cast(unsigned, __builtin_convertvector(f, v2bf));
}
__device__ __forceinline__ float asf(unsigned d) { return __builtin_bit_cast(float, d); }
__device__ __forceinline__ unsigned asu(float f) { return __builtin_bit_cast(unsigned, f); }
__device__ __forceinline__ float blo(unsigned d) { return __builtin_bit_cast(float, d << 16); }
__device__ __forceinline__ float bhi(unsigned d) { return __builtin_bit_cast(float, d & 0xffff0000u); }
__device__ __forceinline__ f32x16 mm32(bf16x8 a, bf16x8 b, f32x16 c) { return __builtin_amdgcn_mfma_f32_32x32x16_bf16(a, b, c, 0, 0, 0); }
__device__ __forceinline__ f32x4 mm16(bf16x8 a, bf16x8 b, f32x4 c) { return __builtin_amdgcn_mfma_f32_16x16x32_bf16(a, b, c, 0, 0, 0); }
__device__ __forceinline__ bf16x8 asb(v4u v) { return __builtin_bit_cast(bf16x8, v); }
template <bool MAX> __device__ __forceinline__ float rows4(float v) {
  {
    const auto r = __builtin_amdgcn_permlane16_swap(asu(v), asu(v), false, false);
    const unsigned a = r[0], b = r[1];
    v = MAX ? __builtin_fmaxf(asf(a), asf(b)) : asf(a) + asf(b);
  }
  const auto r = __builtin_amdgcn_permlane32_swap(asu(v), asu(v), false, false);
  const unsigned a = r[0], b = r[1];
  return MAX ? __builtin_fmaxf(asf(a), asf(b)) : asf(a) + asf(b);
}
template <int B> __device__ __forceinline__ void raise(unsigned foff, rsrc_t rs, u64 tv, unsigned c) {
  asm volatile("s_bitcmp0_b32 %3, %4\n\ts_cbranch_scc1 1f\n\ts_mov_b64 exec, 1\n\tbuffer_store_dwordx2 %0, off, %1, %2 sc1\n\ts_mov_b64 exec, -1\n1:"
               ::"v"(tv), "s"(rs), "s"(foff), "s"(c), "i"(B) : "memory", "scc");
}
template <int B> __device__ __forceinline__ void ld8c(u64& d, unsigned voff, rsrc_t rs, unsigned c) {
  asm volatile("s_bitcmp0_b32 %3, %4\n\ts_cbranch_scc1 1f\n\tbuffer_load_dwordx2 %0, %1, %2, 0 offen sc1\n1:" : "=&v"(d) : "v"(voff), "s"(rs), "s"(c), "i"(B) : "memory", "scc");
}
template <int N, int B> __device__ __forceinline__ void poll(u64 v, unsigned voff, rsrc_t rs, u64 tag, unsigned c) {
  unsigned n = 1u << 19;
  asm volatile("s_bitcmp0_b32 %5, %7\n\ts_cbranch_scc1 2f\n\ts_waitcnt vmcnt(%6)\n1:\n\tv_cmp_ne_u64 vcc, %4, %0\n\ts_cbranch_vccz 2f\n\ts_sleep 3\n\t"
               "buffer_load_dwordx2 %0, %2, %3, 0 offen sc1\n\ts_sub_u32 %1, %1, 1\n\ts_waitcnt vmcnt(0)\n\ts_cbranch_scc0 1b\n2:"
               : "+v"(v), "+s"(n) : "v"(voff), "s"(rs), "s"(tag), "s"(c), "i"(N), "i"(B) : "memory", "vcc", "scc");
}
template <int P> __device__ __forceinline__ void arrive(unsigned cnt, unsigned foff, rsrc_t rs, u64 tv) {
  unsigned a = cnt, t;
  asm volatile("s_setprio 3\n\ts_mov_b64 exec, 1\n\tv_mov_b32 %0, 1\n\tds_add_rtn_u32 %0, %1, %0\n\ts_waitcnt lgkmcnt(0)\n\t"
               "v_cmp_eq_u32 vcc, 7, %0\n\ts_cbranch_vccz 1f\n\tbuffer_store_dwordx2 %2, off, %3, %4 sc1\n1:\n\ts_mov_b64 exec, -1\n\ts_setprio %5"
               : "=&v"(t) : "v"(a), "v"(tv), "s"(rs), "s"(foff), "i"(P) : "memory", "vcc");
}
template <int N> __device__ __forceinline__ void peek(u64 v, u64 tag, unsigned lds, unsigned bits) {
  unsigned a = lds, b = bits;
  asm volatile("s_waitcnt vmcnt(%4)\n\tv_cmp_ne_u64 vcc, %1, %0\n\ts_cbranch_vccnz 1f\n\ts_mov_b64 exec, 1\n\tds_or_b32 %2, %3\n\ts_mov_b64 exec, -1\n1:"
               ::"v"(v), "s"(tag), "v"(a), "v"(b), "i"(N) : "memory", "vcc");
}
__device__ __forceinline__ void ld8(u64& d, unsigned voff, rsrc_t rs) {
  asm volatile("buffer_load_dwordx2 %0, %1, %2, 0 offen sc1" : "=v"(d) : "v"(voff), "s"(rs) : "memory");
}
__device__ __forceinline__ void orw(unsigned lds, unsigned bits) {
  unsigned a = lds, v = bits;
  asm volatile("s_mov_b64 exec, 1\n\tds_or_b32 %0, %1\n\ts_mov_b64 exec, -1" ::"v"(a), "v"(v) : "memory");
}
__device__ __forceinline__ void waitw(unsigned lds, unsigned need) {
  unsigned a = lds, t, m, n = 1u << 22;
  asm volatile("1:\n\tds_read_b32 %0, %3\n\ts_waitcnt lgkmcnt(0)\n\tv_readfirstlane_b32 %2, %0\n\ts_andn2_b32 %2, %4, %2\n\ts_cbranch_scc0 2f\n\t"
               "s_sleep 1\n\ts_sub_u32 %1, %1, 1\n\ts_cbranch_scc0 1b\n2:"
               : "=&v"(t), "+s"(n), "=&s"(m) : "v"(a), "s"(need) : "memory", "scc");
}
__device__ __forceinline__ void pollg(v4u& v, unsigned voff, rsrc_t rs, u64 tag) {
  unsigned v1 = v[1], v3 = v[3];
  const unsigned lo = (unsigned)tag, hi = (unsigned)(tag >> 32);
  if (__builtin_amdgcn_ballot_w64(((v1 ^ lo) | (v3 ^ hi)) != 0) == 0) return;
#pragma unroll 1
  for (unsigned n = 1u << 20; n; --n) {
    v = __builtin_amdgcn_raw_buffer_load_b128(rs, voff, 0, AUX_SC1);
    v1 = v[1]; v3 = v[3];
    if (__builtin_amdgcn_ballot_w64(((v1 ^ lo) | (v3 ^ hi)) != 0) == 0) break;
    __builtin_amdgcn_s_sleep(1);
  }
}
__device__ __forceinline__ unsigned hilo(float c) { return pk(c, c - blo(pk(c, 0.f))); }
__device__ __forceinline__ float rbf(float f) { return blo(pk(f, 0.f)); }
#define STAMP(i)
#define CSTAMP(i)
#define STAMPB(i)
#define STAMPP(i)
#define STAMPN(i) STAMP(i)
}

#if defined(__HIP_DEVICE_COMPILE__)
__attribute__((target("no-packed-fp32-ops")))
#endif
__global__ __launch_bounds__(512, 2) void fused_block(const char* __restrict__ x, const char* __restrict__ wqkv, const char* __restrict__ wo,
                                                      const char* __restrict__ wg, const char* __restrict__ wu, const char* __restrict__ wd,
                                                      char* __restrict__ out, char* __restrict__ ws) {
  STAMP(0);
  const unsigned tid = threadIdx.x, lane = tid & 63, wv = tid >> 6, b = blockIdx.x, xl = b & 7, idx = b >> 3;
  unsigned warm0, warm, warm2;
  {
    u64 pc;
    asm volatile("s_getpc_b64 %0" : "=s"(pc));
    touch(warm0, umin((((b & 56u) + wv) * 3 + (tid & 3)) * 128, CODE_BYTES - 128), (const char*)pc);
  }
  const unsigned w = __builtin_amdgcn_readfirstlane(wv);
#define TV(i) ((i) < 16 ? ta[(i) & 15] : tb[(i) & 15])
  const bool typeA = (idx & 3) < 3;
  const unsigned j = typeA ? 3 * (xl & 3) + (idx & 3) : 12 + (xl & 3), p = 8 * (xl >> 2) + (idx >> 2);

  STAMPP(5);
  const unsigned l3 = lane >> 3, l7 = lane & 7, r8 = tid >> 3;
  const unsigned cw = (l7 ^ ((lane >> 2) & 4)) * 16;
  unsigned vx0 = (128 * p + 8 * wv + r8) * 896 + ((l7 ^ (lane >> 4)) * 16), vx1 = (vx0 ^ 64) + (8 * 896 - 1024);
  const unsigned qb0 = typeA ? j : umin(3 * j - 24, 18u);
  unsigned vq = r8 * 2688 + qb0 * 128 + cw, vf = r8 * 3584 + j * 128 + cw, v12 = typeA ? vf : vq;
  unsigned lw2 = w * 2048, lw3 = w * 3072;
  TIE(vx0); TIE(vx1); TIE(vq); TIE(v12); TIES(lw2); TIES(lw3);
  SCHED();
  asm volatile("" ::"s"(x), "s"(wqkv), "s"(wo), "s"(wg), "s"(wu), "s"(wd), "s"(out), "s"(ws));
  const char* b1 = (typeA ? wg : wqkv + 128) - 1024;
  const char* b2 = (typeA ? wu : wqkv + 256) - 2048;
  const unsigned st12 = typeA ? 64 * 3584 : 64 * 2688;
  auto itemA = [&](auto it_) AI {
    constexpr int it = it_, s = it / 5, k = it % 5;
    constexpr unsigned sl = (s & 3) * SLOT;
    if constexpr (k == 0) { setm0<sl + 24576 - s * 128>(lw2); dmag<s * 128>(vx0, x); }
    else if constexpr (k == 1) dmag<s * 128 + 1024>(vx1, x);
    else if constexpr (k == 2) { setm0<sl>(lw3); dmag<0>(vq, wqkv); vq += 64 * 2688; TIE(vq); }
    else if constexpr (k == 3) dmag<1024>(v12, b1);
    else { dmag<2048>(v12, b2); v12 += st12; TIE(v12); }
  };
  sfor<5>([&](auto it) AI { itemA(it); });
  BAR();
  const unsigned ts = 128 * p + 8 * j + wv;
  v4u xrow;
  asm volatile("global_load_dwordx4 %0, %1, %2" : "=v"(xrow) : "v"(ts * 896 + umin(lane, 55u) * 16), "s"(x) : "memory");
  u64 tag;
  asm volatile("s_load_dwordx2 %0, %1, 0x0" : "=s"(tag) : "s"(ws) : "memory");
  v16u ta, tb;
  asm volatile("s_load_dwordx16 %0, %2, 0x0\n\ts_load_dwordx16 %1, %2, 0x40" : "=&s"(ta), "=&s"(tb) : "s"(kTab.v[b * 8 + w]) : "memory");

  STAMPP(6);
  {
    const unsigned n = wv < 2 ? 19 : 7, k = wv < 2 ? wv : wv < 5 ? wv - 2 : wv - 5;
    const unsigned L = umin((b * (wv < 2 ? 2 : 3) + k) * n + umin(lane, n - 1), 9407u);
    touch(warm, L * 128 + (wv < 2 ? 0u : ((L * 43691u) >> 19) * 2048), w < 2 ? wqkv : w < 5 ? wg : wu);
  }
  SCHED();
  STAMPP(7);
  const unsigned rg = wv & 3, set = wv >> 2, rr = rg ^ (2 * set), tok = lane & 31, hh = lane >> 5;
  const unsigned q4 = (lane & 15) >> 2, p4 = lane & 3, sp4 = ((p4 & 1) << 1) | (p4 >> 1);
  const unsigned trl = q4 * 128 + (((lane >> 2) & 4) + sp4) * 8;
  const unsigned aw = trl + ((set ^ (q4 >> 1)) << 6);
  const unsigned ax = (32 * rg + tok) * 128 + (((2 * rr + hh) ^ ((tok >> 1) & 7)) << 4);
  const unsigned selw = (((lane >> 4) ^ (lane >> 2)) & 1) ? 0u : 0x3F803F80u;
  const bf16x8 sel = asb(v4u{selw, selw, selw, selw});
  unsigned bw[4], bx[4];
#pragma unroll
  for (int i = 0; i < 4; ++i) { bw[i] = aw + (2 * (rr ^ i) + hh) * 3072; bx[i] = (ax ^ (i * 32)) + 24576; TIE(bw[i]); TIE(bx[i]); }

  const rsrc_t rs = mkrs(ws, WS_END);
  unsigned vwm;
  const char* bwm = w < 2 ? wg : w < 4 ? wu : w < 7 ? wd : wo;
  const bool p2 = idx < 28, owave = w >= 4;
  const char* w2p = owave ? wo : wd;
  TIES(w2p);
  unsigned vdA, vdB, vdC, awB, a2, vl, oS, aC, la, fo1;
  rsrc_t rw2, rwd;
  unsigned vdl;
  auto setup = [&](auto n_) AI {
    constexpr int n = n_;
    unsigned l = lane;
    TIE(l);
    if constexpr (n == 0) {
      awB = bw[0] - (l >> 5) * 1024 + TV(T_AWB);
      a2 = ((bw[0] & 0x3e7u) ^ ((w & 4) << 4)) + (l & 3) * 8 + (l >> 5) * 1024 + lw2;
      TIE(awB); TIE(a2);
    } else if constexpr (n == 10) {
      oS = (l & 31) * 16 + TV(T_S); aC = (l & 31) * 16 + TV(T_CS); vl = l * 16;
      TIE(oS); TIE(aC); TIE(vl);
    } else if constexpr (n == 11) {
      la = (l & 15) * 16 + (l >> 4) * 512;
      TIE(la);
    } else if constexpr (n == 12) {
      vdl = (l >> 3) * 896 + ((l & 7) ^ ((l >> 2) & 4)) * 16;
      TIE(vdl);
    } else if constexpr (n == 13) {
      vdA = vdl + TV(T_DA); vdC = vdl + TV(T_DC);
      TIE(vdA); TIE(vdC);
    } else if constexpr (n == 14) {
      fo1 = TV(T_F0) + umin(l & 15, TV(T_FE)) * 8 - ((l >> 4) & 1) * TV(T_F1);
      TIE(fo1);
    } else if constexpr (n == 15) {
      rw2 = mkrs(w2p, TV(T_W2N)); rwd = mkrs(wd, 1792 * 896);
    }
  };
  auto issueWh = [&](auto i_, auto hf_) AI {
    constexpr int i = i_, hf = hf_;
    if constexpr (i == 8 && hf == 0) vdB = vdl + TV(T_DB);
    unsigned& v = i < 8 ? vdA : i == 8 ? vdB : vdC;
    if constexpr (i == 18) dmab2c<wunit(i), B_18>(vdC + 32 * 896, vdC + (40 * 896 - 1024), rwd, lw2, TV(T_FLAGS));
    else if constexpr (hf == 0) { setm0<wunit(i)>(lw2); dmab<0>(v, i < 8 ? rw2 : rwd); v += 8 * 896 - 1024; TIE(v); }
    else { dmab<1024>(v, i < 8 ? rw2 : rwd); v += (i < 8 && (i & 1)) ? TV(T_STP) : 8 * 896 + 1024; TIE(v); }
  };
  vf += 1536;
  auto item = [&](auto it_) AI {
    constexpr int it = it_;
    if constexpr (it < 35) itemA(it_);
    else if constexpr (it == 47 || it == 48 || it >= 51) issueWh(IC<(it >= 51)>{}, IC<((it - 47) & 1)>{});
    else {
      constexpr int h = it < 47 ? it - 35 : it - 37;
      if constexpr (!(h & 1)) setm0<bunit(h >> 1)>(lw2);
      dmag<(h & 1) * 1024>(vf, h >= 7 ? wu : wg);
      vf += (h == 6 ? -6 * 64 * 3584 : 64 * 3584) + ((h & 1) ? 1024 : -1024);
      TIE(vf);
    }
  };
  const unsigned odd = j & 1;

  bf16x8 xf[28];
  f32x16 acc[3], accB[2];
  f32x4 cs[3], csB[2];
  unsigned cpk[3];
#pragma unroll
  for (int t = 0; t < 3; ++t) { acc[t] = 0.f; cs[t] = 0.f; }
#pragma unroll
  for (int t = 0; t < 2; ++t) { accB[t] = 0.f; csB[t] = 0.f; }
  bf16x8 wf[3][3], wb[6];
  auto rdX = [&](auto n_) AI {
    constexpr int n = n_, s = n >> 2, i = n & 3;
    if constexpr (s == 2 || s == 6) { bx[i] += 2 * SLOT; TIE(bx[i]); }
    if constexpr (s == 4) { bx[i] -= 2 * SLOT; TIE(bx[i]); }
    xf[n] = *(const LAS bf16x8*)(uintptr_t)(bx[i] + (s & 1) * SLOT);
  };
  auto rdW = [&](auto n_, auto t_) AI {
    constexpr int n = n_, t = t_, s = n >> 2, i = n & 3;
    if constexpr (t == 0 && (s == 2 || s == 6)) { bw[i] += 2 * SLOT; TIE(bw[i]); }
    if constexpr (t == 0 && s == 4) { bw[i] -= 2 * SLOT; TIE(bw[i]); }
    wf[n % 3][t] = ldtr(bw[i] + (s & 1) * SLOT + t * 1024);
  };
  auto rdB = [&](auto u_) AI {
    constexpr int u = u_, i = u & 3, h = u >> 2;
    constexpr unsigned base = h < 4 ? 8 * UNIT : h < 10 ? 0 : 4 * UNIT;
    if constexpr (h == 0) { bw[i] = awB ^ (i * 4096); TIE(bw[i]); }
    if constexpr (h == 4) { bw[i] -= 8 * UNIT; TIE(bw[i]); }
    if constexpr (h == 10) { bw[i] += 4 * UNIT; TIE(bw[i]); }
    constexpr unsigned off = bunit(h >> 1) - base + (h & 1) * 1024;
    wb[u % 6] = ldtr(bw[i] + off);
  };
  v4u stg;
  float rstd, sa = 0.f, sq = 0.f;
  bf16x8 mu, cf[3];

  asm volatile("s_waitcnt lgkmcnt(0)" : "+s"(tag), "+s"(ta), "+s"(tb)::"memory");
  {
    const unsigned L = umin(TV(T_WM) + umin(lane, TV(T_WMN)), TV(T_WMX));
    vwm = L * 128 + (wv < 4 ? (L >> 4) * 1536 + 1536 : 0u);
    TIE(vwm);
  }
  tag += TAG_STEP;
  u64 tagv = tag;
  TIE(tagv);
  STAMPP(8);
  sfor<PRO - 5>([&](auto it) AI { itemA(IC<5 + it>{}); });
  STAMPP(9);
  waitv<PRO - 5 + 2>();
  TIE(warm0);
  BAR();
  STAMP(1); CSTAMP(12);
  sfor<2>([&](auto n) AI { rdX(n); rdW(n, IC<0>{}); rdW(n, IC<1>{}); rdW(n, IC<2>{}); });
  sfor<28>([&](auto n_) AI {
    constexpr int n = n_, s = n >> 2, i = n & 3, c0 = cum(n - 1), c1 = cum(n);
    SCHED();
    if constexpr (i != 2) PRIO(3 - ((i + 2) & 3));
    if constexpr (i != 3 && n > 0) waitl<7>();
    acc[0] = mm32(wf[n % 3][0], xf[n], acc[0]);
    SCHED();
    if constexpr (i == 2) {
      if constexpr (s == 4) { if (tid < 6) *(LAS unsigned*)(uintptr_t)(L_CNT + tid * 4) = 0u; }
      if constexpr (s == 6) {
#pragma unroll
        for (int t = 0; t < 3; ++t) *(LAS unsigned*)(uintptr_t)(aC + (w & 3) * 4 + t * 512) = cpk[t];
      }
      waitl<0>();
      static_assert(c0 > (s < 6 ? 5 * s + 9 : 38), "next stage issued");
      constexpr int y = nops(n - 1) - 1 - rankI(s < 6 ? 5 * s + 9 : 38);
      waitv<s == 5 ? imin(y, nops(n - 1) - nops(G_ST)) : y>();
      if constexpr (s == 0) { TIE(warm); TIE(xrow); }
      if constexpr (s == 5) { static_assert(GW2 < G_ST, "covered"); TIE(warm2); }
#pragma unroll
      for (int t = 0; t < 3; ++t) { TIE(acc[t]); TIE(cs[t]); }
      BAR();
      PRIO(3);
      if constexpr (s == 6) {
#pragma unroll
        for (int t = 0; t < 3; ++t) cf[t] = *(const LAS bf16x8*)(uintptr_t)(aC + t * 512);
      }
    }
    if constexpr (n < 26) {
      rdX(IC<n + 2>{});
      rdW(IC<n + 2>{}, IC<0>{});
      if constexpr (i == 1) rdW(IC<n + 2>{}, IC<1>{});
    } else rdB(IC<2 * (n - 26)>{});
    SCHED();
    if constexpr (i == 0) cs[0] = mm16(sel, wf[n % 3][0], cs[0]);
    if constexpr (n == 27) acc[0] = mm32(cf[0], mu, acc[0]);
    acc[1] = mm32(wf[n % 3][1], xf[n], acc[1]);
    SCHED();
    if constexpr (n < 26) rdW(IC<n + 2>{}, IC<(i == 1 ? 2 : 1)>{}); else rdB(IC<2 * (n - 26) + 1>{});
    if constexpr (c1 > c0) item(IC<c0>{});
    if constexpr (n == 25) { cpk[0] = hilo(cs[0][0]); TIE(cpk[0]); }
    SCHED();
    if constexpr (i == 0) cs[1] = mm16(sel, wf[n % 3][1], cs[1]);
    if constexpr (n == 27) acc[1] = mm32(cf[1], mu, acc[1]);
    acc[2] = mm32(wf[n % 3][2], xf[n], acc[2]);
    SCHED();
    if constexpr (i != 1 && n < 26) rdW(IC<n + 2>{}, IC<2>{});
    if constexpr (c1 > c0 + 1) item(IC<c0 + 1>{});
    if constexpr (n == GW2) touch(warm2, vwm, bwm);
    if constexpr (n >= 2 && n < 6) {
      const unsigned d = lane < 56 ? xrow[n - 2] : 0u;
      const float lo = blo(d), hi = bhi(d);
      sa += lo + hi; sq = __builtin_fmaf(lo, lo, __builtin_fmaf(hi, hi, sq));
      TIE(sa); TIE(sq);
    }
    if constexpr (n >= 6 && n < 9) { sa = wave_sum2<n - 6>(sa); sq = wave_sum2<n - 6>(sq); TIE(sa); TIE(sq); }
    if constexpr (n == 9) {
      const float m = sa * (1.0f / 448.0f);
      const float v = rbf(__builtin_fmaxf(sq * (1.0f / 448.0f) - m * m, 0.f));
      const float r = __builtin_amdgcn_rcpf(rbf(__builtin_amdgcn_sqrtf(rbf(v + 1e-5f))));
      const v4u gr = {pk(-m, -m), (unsigned)tagv, asu(r), (unsigned)(tagv >> 32)};
      __builtin_amdgcn_raw_buffer_store_b128(gr, rs, lane == 63 ? WS_ST + ts * 16 : OOB, 0, AUX_SC1);
    }
    if constexpr (n == 0 || (n >= 10 && n <= 15)) setup(n_);
    if constexpr (n == 23) {
      TIE(stg);
      pollg(stg, oS, rs, tag);
      rstd = asf(stg[2]);
      const unsigned mh = hh ? 0u : stg[0];
      mu = asb(v4u{mh, mh, mh, mh});
      TIE(mu); TIE(rstd);
    }
    if constexpr (n == G_ST) ld16(stg, oS, rs);
    if constexpr (n == 25) { cpk[1] = hilo(cs[1][0]); cpk[2] = hilo(cs[2][0]); TIE(cpk[1]); TIE(cpk[2]); }
    SCHED();
    if constexpr (i == 0) cs[2] = mm16(sel, wf[n % 3][2], cs[2]);
    if constexpr (n == 27) acc[2] = mm32(cf[2], mu, acc[2]);
  });
  SCHED();
  STAMP(2); CSTAMP(13);
  u64 fl;
  v4u qf, k0, k1, v0, v1, hv;
  bf16x8 w2[9][2];
  float sg[16];
  const float k1s = -1.4426950408889634f * rstd, ik2 = __builtin_amdgcn_rcpf(rstd * rstd);
  auto rdW2 = [&](auto i_, bf16x8* d) AI {
    constexpr unsigned U = wunit(decltype(i_)::value & 31) + (decltype(i_)::value >> 5) * 8192;
    unsigned c = a2 + U, e = c ^ 64;
    TIE(c); TIE(e);
    d[0] = ldtr(c); d[1] = ldtr(e);
  };
  const unsigned oQ = vl + TV(T_Q), fl_ = TV(T_FLAGS), fme = TV(T_ME);
  unsigned hd[4];
  float hprev, den;
  sfor<56>([&](auto u_) AI {
    constexpr int u = u_, t = u >= 28, k = u % 28;
    SCHED();
    accB[t] = mm32(wb[u % 6], xf[k], accB[t]);
    SCHED();
    if constexpr (bbar(u)) {
      if constexpr (u == 28 || u == 52) *(LAS unsigned*)(uintptr_t)(aC + (w & 3) * 4 + (u == 52) * 512) = cpk[0];
      waitl<0>();
      if constexpr (u == U_F1) STAMPB(6);
      if constexpr (u == 12) waitc<bsum(0, u) + 2, bsum(0, u), B_U12>(fl_);
      if constexpr (u == U_F1) waitv<bsum(8, u)>();
      if constexpr (u == U_FE) waitv<bsum(U_S3 + 1, u)>();
      if constexpr (u == U_ATT) { STAMPB(8); poll<bsum(U_POLL + 1, U_ATT), B_7>(fl, fo1, rs, tag, fl_); }
      TIE(accB[t]); TIE(csB[t]);
      BAR();
      if constexpr (u == 12) STAMPB(5);
      if constexpr (u == U_ATT) STAMPB(9);
      if constexpr (u == U_F1) STAMPB(7);
      if constexpr (u == U_F1 || u == U_FE) raise<B_6>(fme + (u == U_F1 ? 0 : WS_FE - WS_F1), rs, tagv, fl_);
      if constexpr (u == 28 || u == 52) cf[0] = *(const LAS bf16x8*)(uintptr_t)(aC + (u == 52) * 512);
    }
    if constexpr ((u & 3) == 0) PRIO(3 - (((u + 4) >> 2) & 3));
    if constexpr (breg(u) >= 0) { constexpr int R = breg(u); rdW2(IC<R>{}, w2[R]); SCHED(); }
    if constexpr (u + 4 < 56) rdB(IC<u + 4>{});
    if constexpr (u < 4) {
      hd[2 * (u & 1)] = pk(acc[0][4 * u] * rstd, acc[0][4 * u + 1] * rstd);
      hd[2 * (u & 1) + 1] = pk(acc[0][4 * u + 2] * rstd, acc[0][4 * u + 3] * rstd);
      if constexpr (u & 1) __builtin_amdgcn_raw_buffer_store_b128(v4u{hd[0], hd[1], hd[2], hd[3]}, rs, oQ + (u >> 1) * 1024, 0, AUX_SC1);
      else { TIE(hd[0]); TIE(hd[1]); }
    } else if constexpr (u == 4) {
      if (!typeA) {
#pragma unroll
        for (int e = 0; e < 4; ++e) {
          const int T = 1 + (e >> 1), f = 8 * (e & 1);
          v4u o;
#pragma unroll
          for (int d = 0; d < 4; ++d) o[d] = pk(acc[T][f + 2 * d] * rstd, acc[T][f + 2 * d + 1] * rstd);
          __builtin_amdgcn_raw_buffer_store_b128(o, rs, oQ + T * 4096 + (e & 1) * 1024, 0, AUX_SC1);
        }
      }
    }
    sfor<hfirst(k - 3) - hfirst(k - 4)>([&](auto q_) AI {
      constexpr int hx = hfirst(k - 4) + q_, e = hx >> 1;
      const float g = t ? accB[0][e] : acc[1][e];
      if constexpr (!(hx & 1)) { den = __builtin_fmaf(__builtin_amdgcn_exp2f(g * k1s), ik2, ik2); TIE(den); }
      else if constexpr (t) { sg[e] = g * __builtin_amdgcn_rcpf(den); TIE(sg[e]); }
      else {
        const float hval = g * __builtin_amdgcn_rcpf(den) * acc[2][e];
        if constexpr (e & 1) { hd[(e >> 1) & 3] = pk(hprev, hval); TIE(hd[(e >> 1) & 3]); }
        else { hprev = hval; TIE(hprev); }
      }
    });
    static_assert(hfirst(U_S2 - 3) == 16 && hfirst(U_S3 - 3) == 32, "store slots");
    if constexpr (u == U_S2 || u == U_S3)
      __builtin_amdgcn_raw_buffer_store_b128(v4u{hd[0], hd[1], hd[2], hd[3]}, rs, vl + TV(T_E) + (u == U_S3) * 1024, 0, AUX_SC1);
    if constexpr (u == 25 || u == 51) { cpk[0] = hilo(csB[t][0]); TIE(cpk[0]); }
    if constexpr (u == U_ATT) { ld16s(qf, la, rs, TV(T_AQ)); ld16s(k0, la, rs, TV(T_K0)); }
    if constexpr (u == U_ATT + 1) { ld16s(k1, la, rs, TV(T_K1)); ld16s(v0, la, rs, TV(T_V0)); }
    if constexpr (u == U_ATT + 2) ld16s(v1, la, rs, TV(T_V1));
    if constexpr (bw2(u) >= 0) issueWh(IC<(bw2(u) >> 1)>{}, IC<(bw2(u) & 1)>{});
    if constexpr (u == U_POLL) ld8c<B_7>(fl, fo1, rs, fl_);
    if constexpr (u == 55) ld8c<B_H>(fl, fo1, rs, fl_);
    SCHED();
    if constexpr (u == 0) csB[0] = mm16(sel, wb[0], csB[0]);
    if constexpr (u + 2 < 56 && ((u + 2) & 3) == 0) csB[u + 2 >= 28] = mm16(sel, wb[(u + 2) % 6], csB[u + 2 >= 28]);
    if constexpr (u == 29 || u == 54) accB[u == 54] = mm32(cf[0], mu, accB[u == 54]);
  });
  SCHED();
  PRIO(3);
  STAMP(4); CSTAMP(14);
#pragma unroll
  for (int c = 0; c < 2; ++c) {
#pragma unroll
    for (int e = 0; e < 4; ++e) hv[e] = pk(sg[8 * c + 2 * e] * accB[1][8 * c + 2 * e], sg[8 * c + 2 * e + 1] * accB[1][8 * c + 2 * e + 1]);
    __builtin_amdgcn_raw_buffer_store_b128(hv, rs, vl + TV(T_H) + 24 * 2048 + c * 1024, 0, AUX_SC1);
  }
  SCHED();
  STAMPN(5);
  unsigned lane_ = __builtin_amdgcn_mbcnt_hi(~0u, __builtin_amdgcn_mbcnt_lo(~0u, 0u));
  TIE(lane_);
  const unsigned n16_ = lane_ & 15, g4_ = lane_ >> 4, q4_ = n16_ >> 2, p4_ = lane_ & 3;
  waitv<2 + 2>();
  TIE(qf); TIE(k0); TIE(k1); TIE(v0); TIE(v1);
  {
    if (odd && g4_ == 0) { qf[0] = 0; qf[1] = 0; }
    if (!odd && g4_ == 3) { qf[2] = 0; qf[3] = 0; }
    constexpr unsigned U2 = wunit(2);
    const unsigned scr = U2 + w * 2048;
    *(LAS v4u*)(uintptr_t)(scr + n16_ * 64 + g4_ * 16) = v0;
    *(LAS v4u*)(uintptr_t)(scr + 1024 + n16_ * 64 + g4_ * 16) = v1;
    const f32x4 z4 = 0.f;
    const f32x4 sc0 = mm16(asb(k0), asb(qf), z4), sc1 = mm16(asb(k1), asb(qf), z4);
    bf16x8 vt[2];
#pragma unroll
    for (int t = 0; t < 2; ++t) {
      const unsigned a = scr + (4 * g4_ + q4_) * 64 + (((2 * p4_ + t + odd) * 8) & 63);
      const v4s lo = __builtin_amdgcn_ds_read_tr16_b64_v4i16((LAS v4s*)(uintptr_t)a);
      const v4s hi = __builtin_amdgcn_ds_read_tr16_b64_v4i16((LAS v4s*)(uintptr_t)(a + 1024));
      vt[t] = __builtin_shufflevector(lo, hi, 0, 1, 2, 3, 4, 5, 6, 7);
    }
    const bool par = n16_ & 1, c0 = n16_ >= 4 * g4_, c1 = n16_ >= 4 * g4_ + 2, back = (p | w) != 0;
    const float ninf = -3.0e38f, sc = 0.1889822365046136f * 1.4426950408889634f;
    const float x0 = par ? sc0[1] : sc0[0], x1 = par ? sc0[3] : sc0[2], y0 = par ? sc1[1] : sc1[0], y1 = par ? sc1[3] : sc1[2];
    float s0 = (c0 ? y0 : x0) * sc, s1 = (c1 ? y1 : x1) * sc;
    s0 = (c0 || back) ? s0 : ninf; s1 = (c1 || back) ? s1 : ninf;
    const float mx = rows4<true>(__builtin_fmaxf(s0, s1));
    const float e0 = __builtin_amdgcn_exp2f(s0 - mx), e1 = __builtin_amdgcn_exp2f(s1 - mx);
    const float inv = __builtin_amdgcn_rcpf(rows4<false>(e0 + e1));
    const unsigned pe = pk(e0 * inv, e1 * inv);
    const unsigned u0 = par ? pe << 16 : pe & 0xffffu, u1 = par ? pe & 0xffff0000u : pe >> 16;
    const v4u pf = {c0 ? 0u : u0, c1 ? 0u : u1, c0 ? u0 : 0u, c1 ? u1 : 0u};
    const f32x4 oa = mm16(vt[0], asb(pf), z4), ob = mm16(vt[1], asb(pf), z4);
    v4u ov = {pk(oa[0], oa[1]), pk(oa[2], oa[3]), pk(ob[0], ob[1]), pk(ob[2], ob[3])};
    if (g4_ == 3) { ov[2] = 0; ov[3] = 0; }
    __builtin_amdgcn_raw_buffer_store_b128(ov, rs, la + TV(T_O), 0, AUX_SC1);
  }
  SCHED();
  STAMPN(6);
  const unsigned fo2 = (lane_ & 3) * 8 + TV(T_F2), lnk = L_CNT + (w & 3) * 4;
  waitv<3>();
  sfor<6>([&](auto i) AI { rdW2(IC<3 + i>{}, w2[3 + i]); });
  waitl<0>();
  orw(lnk, owave ? 8u : 16u);
  auto arrive2 = [&](auto P) AI { arrive<P>(L_CNT + 16, fme + (WS_F2 - WS_F1), rs, tagv); };
  auto arrive3 = [&](auto P) AI { arrive<P>(L_CNT + 20, fme + (WS_F3 - WS_F1), rs, tagv); };
  if (!p2) {
    waitv<1>(); arrive2(IC<0>{}); waitv<0>(); arrive3(IC<0>{});
    if (b == 255 && w == 0) {
#pragma unroll 1
      for (int k = 0; k < 4; ++k) poll<0, 0>(~tag, WS_F1 + (k * 4 + g4_) * FSTR + n16_ * 8, rs, tag, 1u);
      raise<0>(0u, rs, tagv, 1u);
    }
    return;
  }

  {
    const unsigned vo = n16_ * 896 + g4_ * 16 + TV(T_OUT), vA = vl + TV(T_A), vB = vl + TV(T_B);
    unsigned sT = HO_T;
    TIES(sT); TIE(a2);
    unsigned awr = w * 2048 + (lane_ >> 5) * 1024 + (lane_ & 31) * 16;
    unsigned ar = (2 * (w & 1) + (g4_ >> 1)) * UNIT + (g4_ & 1) * 2048 + (16 * (w >> 1) + n16_) * 16;
    const unsigned one = (n16_ >> 3) == (g4_ >> 1) ? 0x3F80u << (16 * (n16_ & 1)) : 0u, jj = (n16_ >> 1) & 3;
    bf16x8 eye = asb(v4u{jj == 0 ? one : 0u, jj == 1 ? one : 0u, jj == 2 ? one : 0u, jj == 3 ? one : 0u});
    TIE(awr); TIE(ar); TIE(eye);
    v4u xres;
    f32x16 c2[2][2];
#pragma unroll
    for (int t = 0; t < 4; ++t) c2[t >> 1][t & 1] = 0.f;
    auto mm4 = [&](const bf16x8* wp, v4u* a) AI {
      TIE(a[0]); TIE(a[1]);
      c2[0][0] = mm32(wp[0], asb(a[0]), c2[0][0]);
      c2[0][1] = mm32(wp[0], asb(a[1]), c2[0][1]);
      c2[1][0] = mm32(wp[1], asb(a[0]), c2[1][0]);
      c2[1][1] = mm32(wp[1], asb(a[1]), c2[1][1]);
      SCHED();
    };
    asm volatile("global_load_dwordx4 %0, %1, %2" : "=v"(xres) : "v"(vo), "s"(x) : "memory");
    if (owave) {
      unsigned v = vdl + TV(T_DT);
      u64 fv[13];
      sfor<13>([&](auto i_) AI {
        constexpr int i = i_;
        if constexpr (i == OPRE) { waitv<2 + 2 * OPRE>(); arrive2(IC<3>{}); STAMPN(7); }
        if constexpr (i == OPRE + 2) { waitv<1 + 2 * i>(); arrive3(IC<OPRI>{}); }
        if constexpr (i == 6) waitw(lnk, 16u);
        if constexpr (i < 6) setm0<wunit(2 + i)>(lw2); else setm0<wunit(i - 4) + 8 * UNIT - 8192>(lw2 - 8 * UNIT);
        dmab<0>(v, rwd); v += 8 * 896 - 1024; TIE(v);
        dmab<1024>(v, rwd); v += 8 * 896 + 1024; TIE(v);
        if constexpr (i == 5 || i == 9 || i == 12) ld8(fv[i], fo2, rs);
        if constexpr (i == 9) peek<9>(fv[5], tag, lnk, 32u);
        if constexpr (i == OUP) PRIO(2);
      });
      u64 f3;
      ld8(f3, fo2 + (WS_F3 - WS_F2), rs);
      peek<8>(fv[9], tag, lnk, 32u);
      orw(lnk, 1u);
      waitv<2>();
      orw(lnk, 2u);
      poll<1, 0>(fv[12], fo2, rs, tag, 1u);
      orw(lnk, 32u);
      poll<0, 0>(f3, fo2 + (WS_F3 - WS_F2), rs, tag, 1u);
      STAMPN(8);
      v4u af[8][2];
      sfor<8>([&](auto i_) AI { constexpr int i = i_; ld16x2<(i & 3) * 1024>(af[i][0], af[i][1], vB + (i >> 2) * 4096, rs, sT); });
      sfor<8>([&](auto i_) AI { constexpr int i = i_; waitv<14 - 2 * i>(); mm4(w2[i], af[i]); });
      waitw(lnk, 4u);
    }
    unsigned w_ = w;
    TIES(w_);
    if (w_ < 4) {
      v4u af[28][2];
      bf16x8 wl[2][2];
      auto ld = [&](auto i_) AI {
        constexpr int i = i_, k = i < 12 ? i : i - 12;
        ld16x2<(k & 3) * 1024>(af[i][0], af[i][1], (i < 12 ? vA : vB) + (k >> 2) * 4096, rs, sT);
      };
      PRIO(1);
      poll<4, 0>(fl, fo1, rs, tag, 1u);
      sfor<HPRE>([&](auto i) AI { ld(i); });
      waitv<2 + 2 * HPRE>();
      arrive2(IC<1>{});
      STAMPN(7);
      sfor<WIN - HPRE>([&](auto i) AI { ld(IC<HPRE + i>{}); });
      waitv<1 + 2 * WIN>();
      arrive3(IC<1>{});
      sfor<28>([&](auto i_) AI {
        constexpr int i = i_, nx = i + 1;
        if constexpr (i == P2W) { waitw(lnk, 32u); STAMPN(8); }
        sfor<p2req(i) - p2req(i - 1)>([&](auto k) AI { ld(IC<p2req(i - 1) + k>{}); });
        if constexpr (nx == 9) waitw(lnk, 8u);
        if constexpr (nx == 15) waitw(lnk, 1u);
        if constexpr (nx == 21) waitw(lnk, 2u);
        if constexpr (nx >= 9 && nx < 28) { rdW2(IC<p2piece(nx)>{}, wl[nx & 1]); SCHED(); }
        waitv<2 * (p2req(i) - 1 - i)>();
        mm4(i < 9 ? w2[i < 9 ? i : 0] : wl[i & 1], af[i]);
        if constexpr (i == 20) orw(lnk, 4u);
      });
    }
    STAMPN(9);
#pragma unroll
    for (int nt = 0; nt < 2; ++nt)
#pragma unroll
      for (int tt = 0; tt < 2; ++tt)
#pragma unroll
        for (int c = 0; c < 2; ++c) {
          v4u o;
#pragma unroll
          for (int e = 0; e < 4; ++e) o[e] = pk(c2[nt][tt][8 * c + 2 * e], c2[nt][tt][8 * c + 2 * e + 1]);
          *(LAS v4u*)(uintptr_t)(awr + (2 * nt + c) * UNIT + tt * 512) = o;
          SCHED();
        }
    WAITL0();
    BAR();
    STAMP(10);
    bf16x8 pt[8];
#pragma unroll
    for (int k = 0; k < 8; ++k) pt[k] = *(const LAS bf16x8*)(uintptr_t)(ar + (k >> 2) * 8192 + (k & 1) * 1024 + ((k >> 1) & 1) * 4096);
    SCHED();
    f32x4 sm[2][2];
#pragma unroll
    for (int k = 0; k < 2; ++k)
#pragma unroll
      for (int h = 0; h < 2; ++h) {
        const f32x4 z4 = 0.f;
        sm[k][h] = mm16(eye, pt[4 * k + 2 + h], mm16(eye, pt[4 * k + h], z4));
      }
    TIE(xres);
    float r[8];
#pragma unroll
    for (int e = 0; e < 8; ++e) {
      const float f = sm[0][e >> 2][e & 3], g = f * __builtin_amdgcn_rcpf(1.0f + __builtin_amdgcn_exp2f(f * -1.4426950408889634f));
      r[e] = __builtin_fmaf(sm[1][e >> 2][e & 3], g, (e & 1) ? bhi(xres[e >> 1]) : blo(xres[e >> 1]));
    }
    const v4u o = {pk(r[0], r[1]), pk(r[2], r[3]), pk(r[4], r[5]), pk(r[6], r[7])};
    __builtin_nontemporal_store(o, (GAS v4u*)(out + vo));
    STAMP(11); CSTAMP(15);
  }
}

extern "C" void kernel_launch(void* const* d_in, const int* in_sizes, int n_in, void* d_out, int out_size, void* d_ws, size_t ws_size,
                              hipStream_t stream) {
  (void)in_sizes; (void)n_in; (void)out_size; (void)ws_size;
  (void)hipFuncSetAttribute((const void*)fused_block, hipFuncAttributeMaxDynamicSharedMemorySize, 163840);
  fused_block<<<256, 512, 163840, stream>>>((const char*)d_in[0], (const char*)d_in[1], (const char*)d_in[2], (const char*)d_in[4],
                                            (const char*)d_in[5], (const char*)d_in[6], (char*)d_out, (char*)d_ws);
}
```
